```python
import torch

def _softmax(x, axis=-1):
    return torch.softmax(x, dim=axis)

def _gelu(x):
    return torch.nn.functional.gelu(x, approximate='tanh')

def _silu(x):
    return torch.nn.functional.silu(x)

def _sigmoid(x):
    return torch.sigmoid(x)

def _layernorm(x, axis=-1, eps=1e-05):
    m = x.mean(dim=axis, keepdim=True)
    v = x.var(dim=axis, keepdim=True, unbiased=False)
    return (x - m) / torch.sqrt(v + eps)

def _scatter_add(tgt, idx, src, axis=0):
    return tgt.clone().index_add_(axis, idx, src)

def _segment_sum(src, seg, n):
    out = torch.zeros((n,) + src.shape[1:], dtype=src.dtype)
    return out.index_add_(0, seg, src)

def _pad(x, pw):
    flat = tuple((v for pair in reversed(pw) for v in pair))
    return torch.nn.functional.pad(x, flat)
kernel_name = 'adaLN_cond__memory_aug_recurrent_mem_tr__geglu_ffn__prompt'

def setup_inputs():

    _w = lambda a: (lambda t: t.to(torch.bfloat16) if t.is_floating_point() else t)(torch.as_tensor(a))

    def _A_setup():
        B, S, Dm = (1, 2048, 576)
        H, Dh = (32, 128)
        M, seg = (8, 32)
        seg = min(seg, S)
        S = S // seg * seg
        return {'x': _w((lambda *s: torch.randn(*s, dtype=torch.bfloat16) * 0.05)(B, S, Dm)), 'mem0': _w((lambda *s: torch.randn(*s, dtype=torch.bfloat16) * 0.4)(B, M, Dm)), 'Wq': _w((lambda *s: torch.randn(*s, dtype=torch.bfloat16) * 3.2)(Dm, H, Dh) * Dm ** (-0.5)), 'Wk': _w((lambda *s: torch.randn(*s, dtype=torch.bfloat16) * 3.2)(Dm, H, Dh) * Dm ** (-0.5)), 'Wv': _w((lambda *s: torch.randn(*s, dtype=torch.bfloat16) * 0.4)(Dm, H, Dh) * Dm ** (-0.5)), 'Wo': _w((lambda *s: torch.randn(*s, dtype=torch.bfloat16) * 0.4)(H, Dh, Dm) * (H * Dh) ** (-0.5)), 'seg_len': seg}
    _A_in = _A_setup()

    def _B_setup():
        B, S, D, F = (1, 2048, 576, 4608)
        return {'x': _w((lambda *s: torch.randn(*s, dtype=torch.bfloat16) * 0.05)(B, S, D)), 'w_gate': _w((lambda *s: torch.randn(*s, dtype=torch.bfloat16) * 0.05)(D, F)), 'w_up': _w((lambda *s: torch.randn(*s, dtype=torch.bfloat16) * 0.05)(D, F)), 'w_down': _w((lambda *s: torch.randn(*s, dtype=torch.bfloat16) * 0.05)(F, D))}
    _B_in = _B_setup()
    return {'A_x': _A_in['x'], 'A_mem0': _A_in['mem0'], 'A_Wq': _A_in['Wq'], 'A_Wk': _A_in['Wk'], 'A_Wv': _A_in['Wv'], 'A_Wo': _A_in['Wo'], 'A_seg_len': _A_in['seg_len'], 'B_x': _B_in['x'], 'B_w_gate': _B_in['w_gate'], 'B_w_up': _B_in['w_up'], 'B_w_down': _B_in['w_down'], 'c': _w((lambda *s: torch.randn(*s, dtype=torch.bfloat16) * 0.1)(1, 576) * 8.0), 'w_mod': _w((lambda *s: torch.randn(*s, dtype=torch.bfloat16) * 0.1)(576, 6 * 576) * 576 ** (-0.5) * 8.0)}

def reference(A_x, A_mem0, A_Wq, A_Wk, A_Wv, A_Wo, A_seg_len, B_x, B_w_gate, B_w_up, B_w_down, c, w_mod):

    def _A_ref(x, mem0, Wq, Wk, Wv, Wo, seg_len):
        B, S, Dm = x.shape
        H, Dh = (Wq.shape[1], Wq.shape[2])
        M = mem0.shape[1]
        scale = Dh ** (-0.5)
        n_seg = S // seg_len
        L = M + seg_len
        idx = torch.arange(L)
        mask = (idx[:, None] < M) | (idx[:, None] >= idx[None, :])
        mem = mem0
        outs = []
        for s in range(n_seg):
            xs = x[:, s * seg_len:(s + 1) * seg_len, :].clone()
            h = torch.concatenate([mem, xs], axis=1)
            q = torch.einsum('btd,dhe->bhte', h, Wq)
            k = torch.einsum('btd,dhe->bhte', h, Wk)
            v = torch.einsum('btd,dhe->bhte', h, Wv)
            scores = torch.einsum('bhqe,bhke->bhqk', q, k) * scale
            scores = torch.where(mask[None, None], scores, -1000000000.0)
            p = _softmax(scores, axis=-1)
            o = torch.einsum('bhqk,bhke->bhqe', p, v)
            o = torch.einsum('bhte,hed->btd', o, Wo)
            mem = o[:, :M, :].clone()
            outs.append(o[:, M:, :])
        return torch.concatenate(outs, axis=1)

    def _B_ref(x, w_gate, w_up, w_down):
        return _gelu(x @ w_gate) * (x @ w_up) @ w_down
    x = A_x
    mod = (_silu(c) @ w_mod).reshape(c.shape[0], 6, 1, -1)
    sa, ca, ga, sb, cb, gb = (mod[:, 0].clone(), mod[:, 1].clone(), mod[:, 2].clone(), mod[:, 3].clone(), mod[:, 4].clone(), mod[:, 5].clone())
    h = _layernorm(x, axis=-1) * (1 + ca) + sa
    x = x + ga * _A_ref(h, A_mem0, A_Wq, A_Wk, A_Wv, A_Wo, A_seg_len)
    h = _layernorm(x, axis=-1) * (1 + cb) + sb
    x = x + gb * _B_ref(h, B_w_gate, B_w_up, B_w_down)
    return x
```

```cpp
#include <hip/hip_runtime.h>
#include <hip/hip_bf16.h>

typedef unsigned short u16;
typedef short s4 __attribute__((ext_vector_type(4)));
typedef __bf16 bf8 __attribute__((ext_vector_type(8)));
typedef __bf16 bf16v __attribute__((ext_vector_type(16)));
typedef float f4 __attribute__((ext_vector_type(4)));
typedef float f2 __attribute__((ext_vector_type(2)));
typedef unsigned v2u __attribute__((ext_vector_type(2)));
typedef unsigned v4u __attribute__((ext_vector_type(4)));
typedef unsigned v8u __attribute__((ext_vector_type(8)));
#define LAS __attribute__((address_space(3)))
#define GAS __attribute__((address_space(1)))
typedef LAS unsigned char* ldsp;
typedef __amdgpu_buffer_rsrc_t rsrc_t;

constexpr int DM = 576, NH = 32, EH = 128, NSEG = 64, HE = NH * EH, FF = 4608;
constexpr int LDS_BYTES = 147456 + 8192;

constexpr unsigned FL_ERR = 0, FL_TA = 128, FL_TB = 256, FL_PAR = 384, FL_INIT = 512, FL_MOD = 1024;
constexpr unsigned FL_QK = 4096, FL_VU = 12288, FL_KM = 20480, FL_UM = 28672, FL_P = 36864, FL_X1 = 45056, FL_H = 53248, FL_ACT = 61440;
constexpr unsigned FLAG_BYTES = 61440 + 64 * 256;
constexpr unsigned UM_SLOT = 36u * 16384u;
constexpr unsigned PB_BASE = 1728u << 10, PB_SLOT = 49152u;
constexpr unsigned MEM_BASE = 1824u << 10, MEM_SLOT = 32768u;
constexpr unsigned PH_BASE = 1920u << 10;
constexpr unsigned CAL_BASE = 1984u << 10;
constexpr unsigned WS_QF = 2u << 20, WS_KF = 18u << 20;
constexpr unsigned WS_US = 34u << 20, USEG_STEP = 36u * 32768u;
constexpr unsigned WS_UM = 106u << 20, UMP_STEP = 36u * 8192u;
constexpr unsigned WS_KM = 124u << 20;
constexpr unsigned WS_PF = 128u << 20, PF_STEP = 81920u;
constexpr unsigned WS_HF = 134u << 20, TF_STEP = 36864u;
constexpr unsigned WS_X1 = 137u << 20;
constexpr unsigned WS_ST = 140u << 20, ST_STEP = 8192u;
constexpr unsigned WS_MOD = 141u << 20;
constexpr unsigned WS_ACT = 142u << 20, ACT_STEP = 294912u;
constexpr unsigned WS_END = 161u << 20;
constexpr unsigned SENT = 0xFFFFFFFFu;
constexpr int SPIN_MAX = 100000;

struct Params {
    const u16 *x, *mem0, *Wq, *Wk, *Wv, *Wo, *Wg, *Wu, *Wd, *c, *wmod;
    u16* out; unsigned* flags; unsigned char* ws;
};

__device__ __forceinline__ v2u trread(ldsp p) { s4 r = __builtin_amdgcn_ds_read_tr16_b64_v4i16((LAS s4*)p); return __builtin_bit_cast(v2u, r); }
__device__ __forceinline__ unsigned pkbf(float lo, float hi) {
    typedef __bf16 bf2 __attribute__((ext_vector_type(2)));
    bf2 r; r[0] = (__bf16)lo; r[1] = (__bf16)hi; return __builtin_bit_cast(unsigned, r); }
__device__ __forceinline__ f4 smf(v4u a, v8u b, f4 c, int idx) { return __builtin_amdgcn_smfmac_f32_16x16x64_bf16(__builtin_bit_cast(bf8, a), __builtin_bit_cast(bf16v, b), c, idx, 0, 0); }
__device__ __forceinline__ f4 mf32(v4u a, v4u b, f4 c) { return __builtin_amdgcn_mfma_f32_16x16x32_bf16(__builtin_bit_cast(bf8, a), __builtin_bit_cast(bf8, b), c, 0, 0, 0); }
__device__ __forceinline__ float u2f(unsigned x) { return __builtin_bit_cast(float, x); }
__device__ __forceinline__ unsigned f2u(float x) { return __builtin_bit_cast(unsigned, x); }
__device__ __forceinline__ float blo(unsigned x) { return u2f(x << 16); }
__device__ __forceinline__ float bhi(unsigned x) { return u2f(x & 0xFFFF0000u); }
__device__ __forceinline__ float rbf(float x) { return blo(pkbf(x, 0.f) & 0xFFFFu); }
__device__ __forceinline__ float swadd32(float a, float b) { auto r = __builtin_amdgcn_permlane32_swap(f2u(a), f2u(b), false, false); return u2f(r[0]) + u2f(r[1]); }
__device__ __forceinline__ float max3f(float a, float b, float c) { float r; asm("v_max3_f32 %0, %1, %2, %3" : "=v"(r) : "v"(a), "v"(b), "v"(c)); return r; }
__device__ __forceinline__ float max2f(float a, float b) { float r; asm("v_max_f32 %0, %1, %2" : "=v"(r) : "v"(a), "v"(b)); return r; }
__device__ __forceinline__ float xmax16(float a) { auto r = __builtin_amdgcn_permlane16_swap(f2u(a), f2u(a), false, false); return max2f(u2f(r[0]), u2f(r[1])); }
__device__ __forceinline__ float xmax32(float a) { auto r = __builtin_amdgcn_permlane32_swap(f2u(a), f2u(a), false, false); return max2f(u2f(r[0]), u2f(r[1])); }
__device__ __forceinline__ float xadd16(float a) { auto r = __builtin_amdgcn_permlane16_swap(f2u(a), f2u(a), false, false); return u2f(r[0]) + u2f(r[1]); }
__device__ __forceinline__ float xadd32(float a) { auto r = __builtin_amdgcn_permlane32_swap(f2u(a), f2u(a), false, false); return u2f(r[0]) + u2f(r[1]); }
template <int CTRL> __device__ __forceinline__ float dppf(float v) { return u2f((unsigned)__builtin_amdgcn_update_dpp(0, (int)f2u(v), CTRL, 0xf, 0xf, false)); }
__device__ __forceinline__ float rowsum16(float v) { v += dppf<0x121>(v); v += dppf<0x122>(v); v += dppf<0x124>(v); v += dppf<0x128>(v); return v; }
__device__ __forceinline__ unsigned umax4(v4u x) { return max(max(x[0], x[1]), max(x[2], x[3])); }
__device__ __forceinline__ v8u cat8(v4u a, v4u b) { return (v8u){a[0], a[1], a[2], a[3], b[0], b[1], b[2], b[3]}; }
__device__ __forceinline__ void rdelay(int n) { for (int i = 0; i < n; ++i) asm volatile("s_nop 1"); }
__device__ __forceinline__ rsrc_t mkrs(const void* p, unsigned bytes) { return __builtin_amdgcn_make_buffer_rsrc((void*)p, 0, bytes, 0x00020000); }
__device__ __forceinline__ v4u ldS(rsrc_t r, unsigned off, unsigned so) { return __builtin_amdgcn_raw_buffer_load_b128(r, off, so, 16); }
__device__ __forceinline__ void stS(v4u v, rsrc_t r, unsigned off, unsigned so) { __builtin_amdgcn_raw_buffer_store_b128(v, r, off, so, 16); }
#define WGBAR() do { asm volatile("s_waitcnt lgkmcnt(0)" ::: "memory"); __builtin_amdgcn_s_barrier(); asm volatile("" ::: "memory"); } while (0)
#define LD16(dst, rsrc, off, so) asm volatile("buffer_load_dwordx4 %0, %1, %2, %3 offen sc1" : "=v"(dst) : "v"(off), "s"(rsrc), "s"(so) : "memory")
#define LD16I(dst, rsrc, off, so, imm) asm volatile("buffer_load_dwordx4 %0, %1, %2, %3 offen offset:" #imm " sc1" : "=v"(dst) : "v"(off), "s"(rsrc), "s"(so) : "memory")
#define LD4(dst, rsrc, off, so) asm volatile("buffer_load_dword %0, %1, %2, %3 offen sc1" : "=v"(dst) : "v"(off), "s"(rsrc), "s"(so) : "memory")
#define ST16S(val, rsrc, off, so) asm volatile("buffer_store_dwordx4 %0, %1, %2, %3 offen sc1\n\ts_nop 1" :: "v"(val), "v"(off), "s"(rsrc), "s"(so) : "memory")
#define ST8S(val, rsrc, off, so) asm volatile("buffer_store_dwordx2 %0, %1, %2, %3 offen sc1\n\ts_nop 1" :: "v"(val), "v"(off), "s"(rsrc), "s"(so) : "memory")
#define ST4S(val, rsrc, off, so) asm volatile("buffer_store_dword %0, %1, %2, %3 offen sc1" :: "v"(val), "v"(off), "s"(rsrc), "s"(so) : "memory")
#define VMWAIT0() asm volatile("s_waitcnt vmcnt(0)" ::: "memory")

__device__ __forceinline__ void set_err(const Params& P, unsigned code) { __hip_atomic_fetch_or((GAS unsigned*)P.flags, code, __ATOMIC_RELAXED, __HIP_MEMORY_SCOPE_AGENT); }
__device__ __forceinline__ bool spin_fail(const Params& P, int sp, int l, unsigned code) {
    if ((sp & 1023) != 1023) return false;
    if (__builtin_amdgcn_readfirstlane(__hip_atomic_load((GAS unsigned*)P.flags, __ATOMIC_RELAXED, __HIP_MEMORY_SCOPE_AGENT)) != 0u) return true;
    if (sp > SPIN_MAX) { if (l == 0) set_err(P, code); return true; }
    return false;
}
__device__ __forceinline__ void wait_flags(const Params& P, rsrc_t rf, unsigned off, int cnt, int l, unsigned code) {
    const unsigned o = off + (unsigned)(l < cnt ? l : 0) * 4u;
    for (int sp = 0;; ++sp) {
        unsigned fl; LD4(fl, rf, o, 0u);
        asm volatile("s_waitcnt vmcnt(0)" : "+v"(fl) :: "memory");
        if (__all(fl != 0u)) break;
        if (spin_fail(P, sp, l, code)) break;
        __builtin_amdgcn_s_sleep(2);
    }
}
__device__ __forceinline__ void set_flag(rsrc_t rf, unsigned off) { unsigned one = 1u; ST4S(one, rf, off, 0u); }

constexpr int MEMT_OFF = 0;
constexpr int T_OFF = MEMT_OFF + 11520;
constexpr int KS_OFF = T_OFF + 6144;
constexpr int RED_OFF = KS_OFF + 8192;
constexpr int D_A = 8, D_B = 10, D_V = 10, D_X = 4;

__device__ __forceinline__ v8u bfrag_sp(ldsp stg, int rowbytes, int row0, int cg, int l, int swzmask) {
    const int gp = l >> 4, q = (l & 15) >> 2, p = l & 3;
    v8u r;
#pragma unroll
    for (int J = 0; J < 2; ++J) {
        const int G = 2 * J + (gp >> 1);
        const int d0 = row0 + 16 * G + 4 * (gp & 1) + q, d1 = d0 + 8;
        v2u x = trread(stg + d0 * rowbytes + ((cg ^ (d0 & swzmask)) * 32) + p * 8);
        v2u y = trread(stg + d1 * rowbytes + ((cg ^ (d1 & swzmask)) * 32) + p * 8);
        r[4 * J + 0] = x[0]; r[4 * J + 1] = y[0]; r[4 * J + 2] = x[1]; r[4 * J + 3] = y[1];
    }
    asm volatile("" : "+v"(r));
    return r;
}
__device__ __forceinline__ void stage_w128(const u16* W, int ld, int col0, ldsp L, int t) {
    v4u tmp[18];
#pragma unroll
    for (int i = 0; i < 18; ++i) { int ch = t + 512 * i; int d = ch >> 4, cc = ch & 15; tmp[i] = *(const v4u*)(W + (size_t)d * ld + col0 + cc * 8); }
    __syncthreads();
#pragma unroll
    for (int i = 0; i < 18; ++i) { int ch = t + 512 * i; int d = ch >> 4, cc = ch & 15; *(LAS v4u*)(L + d * 256 + ((cc ^ ((d & 7) << 1)) * 16)) = tmp[i]; }
    __syncthreads();
}
__device__ __forceinline__ void stage_wo64(const u16* Wo, int row0, ldsp L, int t) {
    v4u tmp[9];
#pragma unroll
    for (int i = 0; i < 9; ++i) { int ch = t + 512 * i; int e = ch / 72, cc = ch % 72; tmp[i] = *(const v4u*)(Wo + (size_t)(row0 + e) * DM + cc * 8); }
    __syncthreads();
#pragma unroll
    for (int i = 0; i < 9; ++i) { int ch = t + 512 * i; int e = ch / 72, cc = ch % 72; *(LAS v4u*)(L + e * 1184 + cc * 16) = tmp[i]; }
    __syncthreads();
}
__device__ __forceinline__ void load_mem0(const u16* mem0, ldsp L, int t) {
    for (int i = t; i < 8 * DM; i += 512) { int r = i / DM, d = i % DM; *(LAS u16*)(L + MEMT_OFF + d * 16 + (d >> 4) * 64 + r * 2) = mem0[i]; }
}
#define MEM_GATHER(EXTRA, errcode) \
    for (int sp = 0;; ++sp) { \
        v4u x0, x1 = {0u, 0u, 0u, 0u}; \
        if (EXTRA) LD16(x1, rs, mg1, msl); \
        LD16(x0, rs, mg0, msl); \
        asm volatile("s_waitcnt vmcnt(0)" : "+v"(x0), "+v"(x1) :: "memory"); \
        unsigned m = max(umax4(x0), umax4(x1)); \
        if (__all(m != SENT)) { *(LAS v4u*)mw0 = x0; if (EXTRA) *(LAS v4u*)mw1 = x1; break; } \
        if (spin_fail(P, sp, l, errcode)) break; \
    }
#define POLL_PHASE(off, val, errcode) \
    for (int sp = 0;; ++sp) { unsigned fl; LD4(fl, rs, off, 0u); asm volatile("s_waitcnt vmcnt(0)" : "+v"(fl) :: "memory"); \
        if (__builtin_amdgcn_readfirstlane(fl) == (unsigned)(val)) break; \
        if (spin_fail(P, sp, l, errcode)) break; }

__device__ __forceinline__ unsigned rec_handshake(const Params& P, ldsp L, int t) {
    VMWAIT0();
    __syncthreads();
    if (t < 64) {
        GAS unsigned* f = (GAS unsigned*)P.flags;
        if (t == 0) __hip_atomic_fetch_add(f + FL_INIT / 4, 1u, __ATOMIC_RELAXED, __HIP_MEMORY_SCOPE_AGENT);
        unsigned par = 1u;
        for (int sp = 0;; ++sp) {
            unsigned c = __hip_atomic_load(f + FL_INIT / 4, __ATOMIC_RELAXED, __HIP_MEMORY_SCOPE_AGENT);
            par = __hip_atomic_load(f + FL_PAR / 4, __ATOMIC_RELAXED, __HIP_MEMORY_SCOPE_AGENT);
            if (c >= 68u && par != 0u) break;
            if (sp > SPIN_MAX) { if (t == 0) set_err(P, 0x100u); par = 1u; break; }
            __builtin_amdgcn_s_sleep(2);
        }
        if (t == 0) *(LAS unsigned*)(L + LDS_BYTES - 16) = par - 1u;
    }
    __syncthreads();
    return __builtin_amdgcn_readfirstlane(*(LAS unsigned*)(L + LDS_BYTES - 16));
}

template <int ROLE, bool SCORE>
__device__ __forceinline__ void a_loop(const Params& P, int r, ldsp L, const v8u (&WQ)[9], const v8u (&WK)[9], unsigned par) {
    const int t = threadIdx.x, w = __builtin_amdgcn_readfirstlane(t >> 6), l = t & 63, n = l & 15, g = l >> 4;
    const int h = r;
    const rsrc_t rs = mkrs(P.ws, WS_END), rf = mkrs(P.flags, FLAG_BYTES);
    const unsigned pa = par << 13;
    const int sidx = (n & 8) ? 0xEEEE : 0x4444;
    const int q4 = n >> 2, p4 = n & 3;
    const ldsp memA = L + MEMT_OFF + (16 * g + 8 * (p4 >> 1) + q4) * 16 + g * 64 + (p4 & 1) * 8;
    const ldsp tW = L + T_OFF + (16 * w + n) * 32 + ((16 * w + n) >> 3) * 128 + g * 8;
    const unsigned mg0 = MEM_BASE + t * 16 + pa, mg1 = MEM_BASE + 16384 + l * 16 + pa;
    const ldsp mw0 = L + MEMT_OFF + t * 16 + (t >> 4) * 64, mw1 = L + MEMT_OFF + (512 + l) * 16 + ((512 + l) >> 4) * 64;
    auto pmap = [&](unsigned off) { return PB_BASE + ((off >> 13) << 14) + (off & 8191u) + pa; };
    const ldsp tR = L + T_OFF + (8 * g + q4) * 32 + g * 128 + p4 * 8;
    const unsigned pst0 = pmap((unsigned)(h >> 1) * 1024u + (unsigned)(16 * (2 * (h & 1) + (g >> 1)) + 8 * (g & 1) + (n & 7)) * 16u);
    const unsigned pst1 = pmap((unsigned)(16 + (h >> 3)) * 1024u + (unsigned)(16 * (2 * ((h >> 2) & 1) + ((h & 3) >> 1)) + 8 * (g & 1) + (n & 7)) * 16u + 8u * (h & 1));
    const int md = r * 16 + n;
    const unsigned ms = MEM_BASE + (md < 512 ? md * 16 : 16384 + (md - 512) * 16) + (g & 1) * 8 + (g >> 1) * 4 + pa;
    const int f0 = 3 * (w - 1);
    const unsigned pl0 = pmap((unsigned)f0 * 1024u + l * 16), pl1 = pmap((unsigned)(f0 + 1) * 1024u + l * 16), pl2 = pmap((unsigned)(f0 + 2) * 1024u + l * 16);
    const unsigned usofs = WS_US + (unsigned)r * 32768u + (unsigned)(ROLE == 1 ? f0 : 15) * 2048u + l * 16;
    const unsigned umofs = (unsigned)r * 16384u + (w == 7 ? 4096u : 0u) + l * 16 + pa;
    const unsigned kofs = WS_KF + (unsigned)h * 8192u + (unsigned)(w & 1) * 4096u + l * 16;
    const ldsp kl = L + KS_OFF + (w & 1) * 4096 + l * 16;
    const float sc2 = 0.08838834764831845f * 1.4426950408889634f;
    for (int s = 0; s < NSEG; ++s) {
        const unsigned psl = (unsigned)(s & 1) * PB_SLOT;
        v4u b0, b1, b2, b3, b4, b5;
        const unsigned uso = (unsigned)s * USEG_STEP;
        const unsigned kso = (unsigned)(s + 1 < NSEG ? s + 1 : s) * (NH * 8192u);
        v4u kf[2][4];
#define PF(i) do { __builtin_amdgcn_sched_barrier(0); \
            if (ROLE == 1) { if (i == 0) LD16I(b0, rs, usofs, uso, 0); if (i == 1) LD16I(b1, rs, usofs, uso, 1024); if (i == 2) LD16I(b2, rs, usofs, uso, 2048); \
                             if (i == 3) LD16I(b3, rs, usofs, uso, 3072); if (i == 4) LD16I(b4, rs, usofs, uso + 4096, 0); if (i == 5) LD16I(b5, rs, usofs, uso + 4096, 1024); } \
            if (ROLE == 2) { if (i == 0) LD16I(b0, rs, usofs, uso, 0); if (i == 1) LD16I(b1, rs, usofs, uso, 1024); \
                             if (SCORE) { if (i == 2) LD16I(b2, rs, kofs, kso, 0); if (i == 3) LD16I(b3, rs, kofs, kso, 1024); if (i == 4) LD16I(b4, rs, kofs, kso, 2048); if (i == 5) LD16I(b5, rs, kofs, kso, 3072); } } \
            __builtin_amdgcn_sched_barrier(0); } while (0)
        if (SCORE) {
            v4u A[9];
#pragma unroll
            for (int kb = 0; kb < 9; ++kb) { v2u x = trread(memA + kb * 1280), y = trread(memA + kb * 1280 + 64); A[kb] = (v4u){x[0], x[1], y[0], y[1]}; }
            __builtin_amdgcn_sched_barrier(0);
            f4 aq = {0.f, 0.f, 0.f, 0.f}, ak = aq;
            PF(0);
            aq = smf(A[0], WQ[0], aq, sidx); ak = smf(A[0], WK[0], ak, sidx); PF(1);
            aq = smf(A[1], WQ[1], aq, sidx); ak = smf(A[1], WK[1], ak, sidx); PF(2);
            if (ROLE == 0) {
#pragma unroll
                for (int u = 0; u < 4; ++u) { const ldsp kp = L + KS_OFF + l * 16 + u * 1024; kf[0][u] = *(LAS v4u*)kp; kf[1][u] = *(LAS v4u*)(kp + 4096); }
            }
            aq = smf(A[2], WQ[2], aq, sidx); ak = smf(A[2], WK[2], ak, sidx);
            aq = smf(A[3], WQ[3], aq, sidx); ak = smf(A[3], WK[3], ak, sidx); PF(3);
            if (ROLE == 0) {
                const v4u ones = {SENT, SENT, SENT, SENT}; const v2u ones2 = {SENT, SENT};
                if (n < 8) { ST16S(ones, rs, pst0, psl ^ PB_SLOT); if (g >= 2) ST8S(ones2, rs, pst1, psl ^ PB_SLOT); }
                __builtin_amdgcn_sched_barrier(0);
            }
            aq = smf(A[4], WQ[4], aq, sidx); ak = smf(A[4], WK[4], ak, sidx);
            aq = smf(A[5], WQ[5], aq, sidx); ak = smf(A[5], WK[5], ak, sidx); PF(4);
            aq = smf(A[6], WQ[6], aq, sidx); ak = smf(A[6], WK[6], ak, sidx);
            aq = smf(A[7], WQ[7], aq, sidx); ak = smf(A[7], WK[7], ak, sidx); PF(5);
            aq = smf(A[8], WQ[8], aq, sidx); ak = smf(A[8], WK[8], ak, sidx);
            {
                float x0 = swadd32(aq[0], ak[0]), x1 = swadd32(aq[1], ak[1]), x2 = swadd32(aq[2], ak[2]), x3 = swadd32(aq[3], ak[3]);
                v2u qk = {pkbf(x0, x1), pkbf(x2, x3)};
                *(LAS v2u*)tW = qk;
            }
            WGBAR();
            if (ROLE == 1 && w == 5) {
                if (h < 4) { unsigned fv = s; if (l == 0) ST4S(fv, rs, PH_BASE + pa + h * 128, 0u); }
                const unsigned ko = WS_KM + (unsigned)h * 2048u + (unsigned)(g * 8 + (n & 7)) * 16u;
#pragma unroll
                for (int u = 0; u < 4; ++u) {
                    v2u x = trread(tR + u * 1536), y = trread(tR + u * 1536 + 128);
                    v4u X = {x[0], x[1], y[0], y[1]};
                    if (n >= 8) ST16S(X, rs, ko, (unsigned)s * (NH * 2048u) + u * 512u);
                }
                VMWAIT0();
                if (l == 0) set_flag(rf, FL_KM + s * 128 + h * 4);
            }
        } else { PF(0); PF(1); PF(2); PF(3); PF(4); PF(5); }
        if (s + 1 == NSEG) break;
        if (ROLE == 0) {
            if (SCORE) {
                f4 sm = {0.f, 0.f, 0.f, 0.f}, s0 = sm, s1 = sm;
                v4u X[4];
#pragma unroll
                for (int u = 0; u < 4; ++u) { v2u x = trread(tR + u * 1536), y = trread(tR + u * 1536 + 128); X[u] = (v4u){x[0], x[1], y[0], y[1]}; }
#pragma unroll
                for (int u = 0; u < 4; ++u) { s0 = mf32(kf[0][u], X[u], s0); s1 = mf32(kf[1][u], X[u], s1); sm = mf32(X[u], X[u], sm); }
                const float NINF = -__builtin_inff();
                if (g < 2) sm = (f4){NINF, NINF, NINF, NINF};
                float mx = max3f(max3f(s0[0], s0[1], s0[2]), max3f(s0[3], s1[0], s1[1]), max3f(s1[2], s1[3], sm[0]));
                mx = max3f(mx, sm[1], max2f(sm[2], sm[3]));
                mx = xmax16(mx); mx = xmax32(mx);
                const float mb = -mx * sc2;
                float e0[4], e1[4], em[4];
#pragma unroll
                for (int q = 0; q < 4; ++q) { e0[q] = __builtin_amdgcn_exp2f(fmaf(s0[q], sc2, mb)); e1[q] = __builtin_amdgcn_exp2f(fmaf(s1[q], sc2, mb)); em[q] = __builtin_amdgcn_exp2f(fmaf(sm[q], sc2, mb)); }
                float sum = ((e0[0] + e0[1]) + (e0[2] + e0[3])) + ((e1[0] + e1[1]) + (e1[2] + e1[3])) + ((em[0] + em[1]) + (em[2] + em[3]));
                sum = xadd16(sum); sum = xadd32(sum);
                const float ri = __builtin_amdgcn_rcpf(sum);
                unsigned p0 = pkbf(e0[0] * ri, e0[1] * ri), p1 = pkbf(e0[2] * ri, e0[3] * ri), p2 = pkbf(e1[0] * ri, e1[1] * ri), p3 = pkbf(e1[2] * ri, e1[3] * ri);
                unsigned m0 = pkbf(em[0] * ri, em[1] * ri), m1 = pkbf(em[2] * ri, em[3] * ri);
                auto ra = __builtin_amdgcn_permlane16_swap(p0, p1, false, false);
                auto rb = __builtin_amdgcn_permlane16_swap(p2, p3, false, false);
                auto rm = __builtin_amdgcn_permlane16_swap(m0, m1, false, false);
                v4u pv = {min(ra[0], SENT - 1), min(rb[0], SENT - 1), min(ra[1], SENT - 1), min(rb[1], SENT - 1)};
                v2u mv = {min(rm[0], SENT - 1), min(rm[1], SENT - 1)};
                VMWAIT0();
                if (n < 8) { ST16S(pv, rs, pst0, psl); if (g >= 2) ST8S(mv, rs, pst1, psl); }
                rdelay(D_A);
            } else {
                const unsigned fo = PH_BASE + pa + (r & 31) * 128; POLL_PHASE(fo, s, 128u); rdelay(D_X);
            }
            WGBAR();
            if (SCORE) { unsigned fv = s; if (l == 0) ST4S(fv, rs, PH_BASE + pa + 4096 + h * 128, 0u); }
        } else {
            asm volatile("s_waitcnt vmcnt(0)" : "+v"(b0), "+v"(b1), "+v"(b2), "+v"(b3), "+v"(b4), "+v"(b5) :: "memory");
            if (ROLE == 2 && SCORE) { *(LAS v4u*)kl = b2; *(LAS v4u*)(kl + 1024) = b3; *(LAS v4u*)(kl + 2048) = b4; *(LAS v4u*)(kl + 3072) = b5; }
            if (ROLE == 2) {
                if (w == 6) wait_flags(P, rf, FL_VU + (s + 1) * 128, 32, l, 4u);
                else if (SCORE && s + 2 < NSEG) wait_flags(P, rf, FL_QK + (s + 2) * 128 + h * 4, 1, l, 8u);
            }
            WGBAR();
        }
        const unsigned msl = (unsigned)((s + 1) % 3) * MEM_SLOT;
        if (ROLE != 0) {
            f4 acc;
            const unsigned umo = (unsigned)(s % 3) * UM_SLOT;
            for (int sp = 0;; ++sp) {
                v4u a0, a1, a2 = {0u, 0u, 0u, 0u};
                if (ROLE == 2) { LD16I(b2, rs, umofs, umo, 0); LD16I(b3, rs, umofs, umo, 1024); LD16I(b4, rs, umofs, umo, 2048); LD16I(b5, rs, umofs, umo, 3072); }
                LD16(a0, rs, pl0, psl); LD16(a1, rs, pl1, psl);
                if (ROLE == 1 || w == 6) LD16(a2, rs, pl2, psl);
                asm volatile("s_waitcnt vmcnt(0)" : "+v"(a0), "+v"(a1), "+v"(a2), "+v"(b2), "+v"(b3), "+v"(b4), "+v"(b5) :: "memory");
                acc = (f4){0.f, 0.f, 0.f, 0.f};
                unsigned m = max(max(umax4(a0), umax4(a1)), umax4(a2));
                if (ROLE == 1 || w == 6) { acc = smf(a0, cat8(b0, b1), acc, sidx); acc = smf(a1, cat8(b2, b3), acc, sidx); acc = smf(a2, cat8(b4, b5), acc, sidx); }
                else { acc = smf(a0, cat8(b2, b3), acc, sidx); acc = smf(a1, cat8(b4, b5), acc, sidx); }
                if (ROLE == 2) m = max(max(m, max(umax4(b2), umax4(b3))), max(umax4(b4), umax4(b5)));
                if (__all(m != SENT)) break;
                if (spin_fail(P, sp, l, 1u)) break;
            }
            f2 hs = {swadd32(acc[0], acc[1]), swadd32(acc[2], acc[3])};
            *(LAS f2*)(L + RED_OFF + w * 512 + l * 8) = hs;
        }
        WGBAR();
        if (ROLE == 0) {
            f2 v[7];
#pragma unroll
            for (int i = 0; i < 7; ++i) v[i] = *(LAS f2*)(L + RED_OFF + (i + 1) * 512 + l * 8);
            f2 sum = ((v[0] + v[1]) + (v[2] + v[3])) + ((v[4] + v[5]) + v[6]);
            auto rr = __builtin_amdgcn_permlane32_swap(f2u(sum[0]), f2u(sum[1]), false, false);
            unsigned pk = min(pkbf(u2f(rr[0]), u2f(rr[1])), SENT - 1);
            if (!SCORE) VMWAIT0();
            ST4S(pk, rs, ms, msl);
            ST4S(SENT, rs, ms, (unsigned)(s % 3) * MEM_SLOT);
            if (SCORE) rdelay(D_B);
        }
        if (SCORE) {
            WGBAR();
            MEM_GATHER((ROLE == 2 && w == 7), 2u);
        }
        WGBAR();
    }
#undef PF
    VMWAIT0();
}

__device__ __forceinline__ void a_body(const Params& P, int r, ldsp L) {
    const int t = threadIdx.x, w = __builtin_amdgcn_readfirstlane(t >> 6), l = t & 63;
    const rsrc_t rs = mkrs(P.ws, WS_END), rf = mkrs(P.flags, FLAG_BYTES);
    if (r == 0 && t == 0) {
        unsigned best[2];
#pragma unroll
        for (int p = 0; p < 2; ++p) {
            GAS unsigned* a = (GAS unsigned*)(P.ws + CAL_BASE + p * 8192);
            unsigned b = 1u << 30;
            for (int i = 0; i < 4; ++i) {
                unsigned long long t0 = __builtin_amdgcn_s_memtime();
                __hip_atomic_store(a, (unsigned)i, __ATOMIC_RELAXED, __HIP_MEMORY_SCOPE_AGENT);
                VMWAIT0();
                unsigned d = (unsigned)(__builtin_amdgcn_s_memtime() - t0); b = d < b ? d : b;
            }
            best[p] = b;
        }
        __hip_atomic_store((GAS unsigned*)P.flags + FL_PAR / 4, best[0] < best[1] ? 1u : 2u, __ATOMIC_RELAXED, __HIP_MEMORY_SCOPE_AGENT);
    }
    {
        const v4u ones = {SENT, SENT, SENT, SENT};
        const unsigned share = 7296u;
        unsigned o = (unsigned)r * share + t * 16;
        if ((unsigned)t * 16u < share && o < (CAL_BASE - PB_BASE)) stS(ones, rs, PB_BASE + o, 0);
    }
    v8u WQ[9], WK[9];
    if (r < 32) {
        stage_w128(P.Wq, HE, r * EH, L, t);
#pragma unroll
        for (int kb = 0; kb < 9; ++kb) WQ[kb] = bfrag_sp(L, 256, kb * 64, w, l, 7);
        stage_w128(P.Wk, HE, r * EH, L, t);
#pragma unroll
        for (int kb = 0; kb < 9; ++kb) WK[kb] = bfrag_sp(L, 256, kb * 64, w, l, 7);
        __syncthreads();
        load_mem0(P.mem0, L, t);
        const unsigned par = rec_handshake(P, L, t);
        if (w == 0) wait_flags(P, rf, FL_VU, 32, l, 16u);
        if (w == 1) wait_flags(P, rf, FL_QK + r * 4, 1, l, 16u);
        if (w == 2) wait_flags(P, rf, FL_QK + 128 + r * 4, 1, l, 16u);
        __syncthreads();
        *(LAS v4u*)(L + KS_OFF + t * 16) = ldS(rs, WS_KF + (unsigned)r * 8192u + t * 16, 0);
        __syncthreads();
        if (w == 0) a_loop<0, true>(P, r, L, WQ, WK, par); else if (w < 6) a_loop<1, true>(P, r, L, WQ, WK, par); else a_loop<2, true>(P, r, L, WQ, WK, par);
    } else {
        const unsigned par = rec_handshake(P, L, t);
        if (w == 0) wait_flags(P, rf, FL_VU, 32, l, 16u);
        __syncthreads();
        if (w == 0) a_loop<0, false>(P, r, L, WQ, WK, par); else if (w < 6) a_loop<1, false>(P, r, L, WQ, WK, par); else a_loop<2, false>(P, r, L, WQ, WK, par);
    }
}

template <bool FIVE>
__device__ __forceinline__ void b_loop(const Params& P, int h, ldsp L, const v8u (&WV)[9], const v8u (&WO)[5][2], unsigned par) {
    const int t = threadIdx.x, w = __builtin_amdgcn_readfirstlane(t >> 6), l = t & 63, n = l & 15, g = l >> 4;
    const rsrc_t rs = mkrs(P.ws, WS_END), rf = mkrs(P.flags, FLAG_BYTES);
    const unsigned pa = par << 13;
    const int sidx = (n & 8) ? 0xEEEE : 0x4444;
    const int q4 = n >> 2, p4 = n & 3;
    const int aoff = (16 * g + 8 * (p4 >> 1) + q4) * 16 + g * 64 + (p4 & 1) * 8;
    const ldsp memA = L + MEMT_OFF + aoff, vmA = L + T_OFF + aoff;
    const ldsp vmW = L + T_OFF + (16 * w + n) * 16 + w * 64 + (g & 1) * 8 + (g >> 1) * 4;
    const unsigned mg0 = MEM_BASE + t * 16 + pa, mg1 = MEM_BASE + 16384 + l * 16 + pa;
    const ldsp mw0 = L + MEMT_OFF + t * 16 + (t >> 4) * 64, mw1 = L + MEMT_OFF + (512 + l) * 16 + ((512 + l) >> 4) * 64;
    const int ptile = 4 * w + ((g & 1) << 1) + (g >> 1);
    const unsigned us0 = (unsigned)ptile * 16384u + h * 256 + n * 16 + pa;
    const unsigned us1 = (unsigned)(32 + (w & 3)) * 16384u + h * 256 + n * 16 + (g & 1) * 8 + (g >> 1) * 4 + pa;
    const unsigned up0 = WS_UM + (unsigned)ptile * 8192u + h * 256 + n * 16;
    const unsigned up1 = WS_UM + (unsigned)(32 + (w & 3)) * 8192u + h * 256 + n * 16 + (g & 1) * 8 + (g >> 1) * 4;
    for (int s = 0; s < NSEG; ++s) {
        const unsigned umo = (unsigned)(s % 3) * UM_SLOT;
        v4u A[9];
#pragma unroll
        for (int kb = 0; kb < 9; ++kb) { v2u x = trread(memA + kb * 1280), y = trread(memA + kb * 1280 + 64); A[kb] = (v4u){x[0], x[1], y[0], y[1]}; }
        __builtin_amdgcn_sched_barrier(0);
        f4 av = {0.f, 0.f, 0.f, 0.f};
#pragma unroll
        for (int kb = 0; kb < 9; ++kb) av = smf(A[kb], WV[kb], av, sidx);
        { float s01 = swadd32(av[0], av[1]), s23 = swadd32(av[2], av[3]);
          auto rr = __builtin_amdgcn_permlane32_swap(f2u(s01), f2u(s23), false, false);
          *(LAS unsigned*)vmW = pkbf(u2f(rr[0]), u2f(rr[1])); }
        WGBAR();
        v4u ov; unsigned pe = 0u;
        {
            v4u oa[2];
#pragma unroll
            for (int kb = 0; kb < 2; ++kb) { v2u x = trread(vmA + kb * 1280), y = trread(vmA + kb * 1280 + 64); oa[kb] = (v4u){x[0], x[1], y[0], y[1]}; }
            f4 z = {0.f, 0.f, 0.f, 0.f};
            f4 d[5];
#pragma unroll
            for (int i = 0; i < (FIVE ? 5 : 4); ++i) { d[i] = smf(oa[0], WO[i][0], z, sidx); }
#pragma unroll
            for (int i = 0; i < (FIVE ? 5 : 4); ++i) { d[i] = smf(oa[1], WO[i][1], d[i], sidx); }
            unsigned xp0 = pkbf(swadd32(d[0][0], d[1][0]), swadd32(d[0][1], d[1][1])), xp1 = pkbf(swadd32(d[0][2], d[1][2]), swadd32(d[0][3], d[1][3]));
            unsigned yp0 = pkbf(swadd32(d[2][0], d[3][0]), swadd32(d[2][1], d[3][1])), yp1 = pkbf(swadd32(d[2][2], d[3][2]), swadd32(d[2][3], d[3][3]));
            auto r0 = __builtin_amdgcn_permlane16_swap(xp0, yp0, false, false);
            auto r1 = __builtin_amdgcn_permlane16_swap(xp1, yp1, false, false);
            ov = (v4u){min(r0[0], SENT - 1), min(r1[0], SENT - 1), min(r0[1], SENT - 1), min(r1[1], SENT - 1)};
            if (s + 1 < NSEG) ST16S(ov, rs, us0, umo);
            if (FIVE) {
                float s01 = swadd32(d[4][0], d[4][1]), s23 = swadd32(d[4][2], d[4][3]);
                auto rr = __builtin_amdgcn_permlane32_swap(f2u(s01), f2u(s23), false, false);
                pe = min(pkbf(u2f(rr[0]), u2f(rr[1])), SENT - 1);
                if (s + 1 < NSEG) ST4S(pe, rs, us1, umo);
            }
        }
        if (s + 1 < NSEG) {
            if (FIVE && w == 0) { const unsigned fo = PH_BASE + pa + 4096 + h * 128; POLL_PHASE(fo, s, 64u); }
            WGBAR();
        }
        ST16S(ov, rs, up0, (unsigned)s * UMP_STEP); if (FIVE) ST4S(pe, rs, up1, (unsigned)s * UMP_STEP);
        if (s + 1 == NSEG) {
            VMWAIT0(); WGBAR();
            if (t == 0) set_flag(rf, FL_UM + s * 128 + h * 4);
            break;
        }
        {
            const unsigned umr = (unsigned)((s + 2) % 3) * UM_SLOT;
            const v4u ones = {SENT, SENT, SENT, SENT};
            ST16S(ones, rs, us0, umr); if (FIVE) ST4S(ones[0], rs, us1, umr);
        }
        if (FIVE && w == 0) rdelay(D_V);
        WGBAR();
        const unsigned msl = (unsigned)((s + 1) % 3) * MEM_SLOT;
        MEM_GATHER((!FIVE && w == 7), 32u);
        WGBAR();
        if (t == 0) set_flag(rf, FL_UM + s * 128 + h * 4);
    }
    VMWAIT0();
}

__device__ __forceinline__ void b_body(const Params& P, int h, ldsp L) {
    const int t = threadIdx.x, w = __builtin_amdgcn_readfirstlane(t >> 6), l = t & 63;
    const rsrc_t rs = mkrs(P.ws, WS_END);
    {
        const v4u ones = {SENT, SENT, SENT, SENT};
#pragma unroll
        for (int i = 0; i < 7; ++i) { unsigned o = (unsigned)(i * 512 + t) * 16u; if (o < 55296u) stS(ones, rs, (unsigned)h * 55296u + o, 0); }
    }
    v8u WV[9], WO[5][2];
    stage_w128(P.Wv, HE, h * EH, L, t);
#pragma unroll
    for (int kb = 0; kb < 9; ++kb) WV[kb] = bfrag_sp(L, 256, kb * 64, w, l, 7);
#pragma unroll
    for (int kb = 0; kb < 2; ++kb) {
        stage_wo64(P.Wo, h * EH + kb * 64, L, t);
#pragma unroll
        for (int i = 0; i < 4; ++i) WO[i][kb] = bfrag_sp(L, 1184, 0, 4 * w + i, l, 0);
        WO[4][kb] = bfrag_sp(L, 1184, 0, 32 + (w & 3), l, 0);
    }
    __syncthreads();
    load_mem0(P.mem0, L, t);
    const unsigned par = rec_handshake(P, L, t);
    if (w < 4) b_loop<true>(P, h, L, WV, WO, par); else b_loop<false>(P, h, L, WV, WO, par);
}

__device__ __forceinline__ v4u dfrag(ldsp stg, int rowbytes, int k0, int cb, int pstep, int l, int swzmask) {
    const int g = l >> 4, q = (l & 15) >> 2, p = l & 3;
    const int d0 = k0 + 8 * g + q, d1 = d0 + 4;
    const int cbyte = cb + p * pstep;
    v2u x = trread(stg + d0 * rowbytes + (cbyte ^ ((d0 & swzmask) << 5)));
    v2u y = trread(stg + d1 * rowbytes + (cbyte ^ ((d1 & swzmask) << 5)));
    v4u r = {x[0], x[1], y[0], y[1]};
    asm volatile("" : "+v"(r));
    return r;
}
constexpr int AT_OFF = 0;
constexpr int XC_OFF = 36864;
constexpr int BR_OFF = 49152;

#define TILE_LOAD(T, base) do { _Pragma("unroll") for (int i = 0; i < 4; ++i) T[i] = ldS(rs, (base) + (unsigned)(w + 8 * i) * 1024u + l * 16, 0); \
        if (w < 4) T[4] = ldS(rs, (base) + (unsigned)(32 + w) * 1024u + l * 16, 0); } while (0)
#define TILE_STORE(T) do { _Pragma("unroll") for (int i = 0; i < 4; ++i) *(LAS v4u*)(L + AT_OFF + (w + 8 * i) * 1024 + l * 16) = T[i]; \
        if (w < 4) *(LAS v4u*)(L + AT_OFF + (32 + w) * 1024 + l * 16) = T[4]; } while (0)

__device__ __forceinline__ void qk_body(const Params& P, int h, ldsp L) {
    const int t = threadIdx.x, w = __builtin_amdgcn_readfirstlane(t >> 6), l = t & 63;
    const rsrc_t rs = mkrs(P.ws, WS_END), rf = mkrs(P.flags, FLAG_BYTES);
    const int u = w & 3;
    v4u W[2][18];
#pragma unroll
    for (int m = 0; m < 2; ++m) {
        stage_w128(m == 0 ? P.Wk : P.Wq, HE, h * EH, L, t);
        if ((w >> 2) == m) {
#pragma unroll
            for (int ks = 0; ks < 18; ++ks) { W[0][ks] = dfrag(L, 256, ks * 32, u * 64, 16, l, 7); W[1][ks] = dfrag(L, 256, ks * 32, u * 64 + 8, 16, l, 7); }
        }
    }
    const unsigned ob = ((w >> 2) == 0 ? WS_KF : WS_QF) + (unsigned)h * 8192u + (unsigned)u * 1024u + l * 16;
    v4u T[5];
    if (w == 0) wait_flags(P, rf, FL_H, 1, l, 0x200u);
    __syncthreads();
    TILE_LOAD(T, WS_HF);
    for (int s = 0; s < NSEG; ++s) {
        TILE_STORE(T);
        VMWAIT0();
        if (w == 0 && s + 1 < NSEG) wait_flags(P, rf, FL_H + (s + 1) * 128, 1, l, 0x200u);
        WGBAR();
        if (t == 0 && s > 0) set_flag(rf, FL_QK + (s - 1) * 128 + h * 4);
        if (s + 1 < NSEG) TILE_LOAD(T, WS_HF + (unsigned)(s + 1) * TF_STEP);
        f4 c[2][2];
#pragma unroll
        for (int a = 0; a < 2; ++a) { c[a][0] = (f4){0.f, 0.f, 0.f, 0.f}; c[a][1] = c[a][0]; }
#pragma unroll
        for (int ks = 0; ks < 18; ++ks) {
            v4u b0 = *(LAS v4u*)(L + AT_OFF + ks * 1024 + l * 16), b1 = *(LAS v4u*)(L + AT_OFF + (18 + ks) * 1024 + l * 16);
            c[0][0] = mf32(W[0][ks], b0, c[0][0]); c[1][0] = mf32(W[1][ks], b0, c[1][0]);
            c[0][1] = mf32(W[0][ks], b1, c[0][1]); c[1][1] = mf32(W[1][ks], b1, c[1][1]);
        }
#pragma unroll
        for (int tt = 0; tt < 2; ++tt) {
            v4u o = {pkbf(c[0][tt][0], c[0][tt][1]), pkbf(c[0][tt][2], c[0][tt][3]), pkbf(c[1][tt][0], c[1][tt][1]), pkbf(c[1][tt][2], c[1][tt][3])};
            stS(o, rs, ob + tt * 4096u, (unsigned)s * (NH * 8192u));
        }
        if (s < 2) { VMWAIT0(); WGBAR(); if (t == 0) set_flag(rf, FL_QK + s * 128 + h * 4); }
        else WGBAR();
    }
    VMWAIT0(); WGBAR();
    if (t == 0) set_flag(rf, FL_QK + (NSEG - 1) * 128 + h * 4);
}

template <bool FIVE>
__device__ __forceinline__ void vu_loop(const Params& P, int h, ldsp L, const v4u (&WV)[18], const v4u (&WO)[5][4]) {
    const int t = threadIdx.x, w = __builtin_amdgcn_readfirstlane(t >> 6), l = t & 63;
    const rsrc_t rs = mkrs(P.ws, WS_END), rf = mkrs(P.flags, FLAG_BYTES);
    const unsigned ob = WS_US + (unsigned)(4 * w) * 32768u + (unsigned)h * 1024u + l * 16;
    const unsigned ob5 = WS_US + (unsigned)(32 + (w & 3)) * 32768u + (unsigned)h * 1024u + l * 16;
    v4u T[5];
    TILE_LOAD(T, WS_HF);
    for (int s = 0; s < NSEG; ++s) {
        TILE_STORE(T);
        VMWAIT0();
        if (w == 0 && s + 1 < NSEG) wait_flags(P, rf, FL_H + (s + 1) * 128, 1, l, 0x200u);
        WGBAR();
        if (t == 0 && s > 0) set_flag(rf, FL_VU + (s - 1) * 128 + h * 4);
        if (s + 1 < NSEG) TILE_LOAD(T, WS_HF + (unsigned)(s + 1) * TF_STEP);
        f4 c0 = {0.f, 0.f, 0.f, 0.f}, c1 = c0;
#pragma unroll
        for (int ks = 0; ks < 18; ++ks) {
            v4u b0 = *(LAS v4u*)(L + AT_OFF + ks * 1024 + l * 16), b1 = *(LAS v4u*)(L + AT_OFF + (18 + ks) * 1024 + l * 16);
            c0 = mf32(WV[ks], b0, c0); c1 = mf32(WV[ks], b1, c1);
        }
        { const ldsp xp = L + XC_OFF + (w >> 1) * 1024 + l * 16 + (w & 1) * 8;
          *(LAS v2u*)xp = (v2u){pkbf(c0[0], c0[1]), pkbf(c0[2], c0[3])}; *(LAS v2u*)(xp + 4096) = (v2u){pkbf(c1[0], c1[1]), pkbf(c1[2], c1[3])}; }
        WGBAR();
        v4u A[2][4];
#pragma unroll
        for (int mt = 0; mt < 2; ++mt)
#pragma unroll
            for (int ks = 0; ks < 4; ++ks) A[mt][ks] = *(LAS v4u*)(L + XC_OFF + mt * 4096 + ks * 1024 + l * 16);
#pragma unroll
        for (int i = 0; i < (FIVE ? 5 : 4); ++i) {
            f4 d0 = {0.f, 0.f, 0.f, 0.f}, d1 = d0;
#pragma unroll
            for (int ks = 0; ks < 4; ++ks) { d0 = mf32(A[0][ks], WO[i][ks], d0); d1 = mf32(A[1][ks], WO[i][ks], d1); }
            v4u o = {pkbf(d0[0], d0[1]), pkbf(d0[2], d0[3]), pkbf(d1[0], d1[1]), pkbf(d1[2], d1[3])};
            if (i < 4) stS(o, rs, ob, (unsigned)s * USEG_STEP + (unsigned)i * 32768u); else stS(o, rs, ob5, (unsigned)s * USEG_STEP);
        }
        if (s < 2) { VMWAIT0(); WGBAR(); if (t == 0) set_flag(rf, FL_VU + s * 128 + h * 4); }
    }
    VMWAIT0(); WGBAR();
    if (t == 0) set_flag(rf, FL_VU + (NSEG - 1) * 128 + h * 4);
}
__device__ __forceinline__ void vu_body(const Params& P, int h, ldsp L) {
    const int t = threadIdx.x, w = __builtin_amdgcn_readfirstlane(t >> 6), l = t & 63;
    const rsrc_t rf = mkrs(P.flags, FLAG_BYTES);
    v4u WV[18], WO[5][4];
    stage_w128(P.Wv, HE, h * EH, L, t);
#pragma unroll
    for (int ks = 0; ks < 18; ++ks) WV[ks] = dfrag(L, 256, ks * 32, (w >> 1) * 64 + (w & 1) * 8, 16, l, 7);
#pragma unroll
    for (int kb = 0; kb < 2; ++kb) {
        stage_wo64(P.Wo, h * EH + kb * 64, L, t);
#pragma unroll
        for (int k2 = 0; k2 < 2; ++k2) {
#pragma unroll
            for (int i = 0; i < 4; ++i) WO[i][kb * 2 + k2] = dfrag(L, 1184, k2 * 32, (4 * w + i) * 32, 8, l, 0);
            WO[4][kb * 2 + k2] = dfrag(L, 1184, k2 * 32, (32 + (w & 3)) * 32, 8, l, 0);
        }
    }
    if (w == 0) wait_flags(P, rf, FL_H, 1, l, 0x200u);
    __syncthreads();
    if (w < 4) vu_loop<true>(P, h, L, WV, WO); else vu_loop<false>(P, h, L, WV, WO);
}

__device__ __forceinline__ void mod_slice(const Params& P, int m, ldsp L) {
    const int t = threadIdx.x, kq = t >> 4, cc = t & 15;
    const rsrc_t rs = mkrs(P.ws, WS_END), rf = mkrs(P.flags, FLAG_BYTES);
    v4u wv[18];
#pragma unroll
    for (int i = 0; i < 18; ++i) wv[i] = *(const v4u*)(P.wmod + (size_t)(kq * 18 + i) * (6 * DM) + m * 128 + cc * 8);
    float acc[8] = {0.f, 0.f, 0.f, 0.f, 0.f, 0.f, 0.f, 0.f};
#pragma unroll
    for (int i = 0; i < 18; ++i) {
        float cv = blo((unsigned)P.c[kq * 18 + i]);
        float sv = rbf(cv / (1.f + __expf(-cv)));
#pragma unroll
        for (int j = 0; j < 4; ++j) { acc[2 * j] = fmaf(sv, blo(wv[i][j]), acc[2 * j]); acc[2 * j + 1] = fmaf(sv, bhi(wv[i][j]), acc[2 * j + 1]); }
    }
    * (LAS f4*)(L + kq * 512 + cc * 32) = (f4){acc[0], acc[1], acc[2], acc[3]};
    * (LAS f4*)(L + kq * 512 + cc * 32 + 16) = (f4){acc[4], acc[5], acc[6], acc[7]};
    __syncthreads();
    if (t < 128) {
        float sacc = 0.f;
#pragma unroll
        for (int k = 0; k < 32; ++k) sacc += *(LAS float*)(L + k * 512 + t * 4);
        __builtin_amdgcn_raw_buffer_store_b32(f2u(rbf(sacc)), rs, WS_MOD + (unsigned)(m * 128 + t) * 4u, 0, 16);
    }
    VMWAIT0();
    __syncthreads();
    if (t == 0) set_flag(rf, FL_MOD + m * 4);
}

__device__ __forceinline__ void ln1_body(const Params& P, int i0, int stride, ldsp L) {
    const int t = threadIdx.x, w = __builtin_amdgcn_readfirstlane(t >> 6), l = t & 63, j = l & 15;
    const rsrc_t rs = mkrs(P.ws, WS_END), rf = mkrs(P.flags, FLAG_BYTES);
    const int row = 4 * w + (l >> 4), tt = row >> 4, n = row & 15;
    bool have_mod = false;
    constexpr int MOD_OFF = 40960;
    for (int s = i0; s < NSEG; s += stride) {
        v4u xr[5];
        const u16* xp = P.x + (size_t)(s * 32 + row) * DM;
#pragma unroll
        for (int i = 0; i < 4; ++i) xr[i] = *(const v4u*)(xp + (j + 16 * i) * 8);
        xr[4] = (v4u){0u, 0u, 0u, 0u};
        if (j < 8) xr[4] = *(const v4u*)(xp + (64 + j) * 8);
        float sum = 0.f;
#pragma unroll
        for (int i = 0; i < 5; ++i)
#pragma unroll
            for (int k = 0; k < 4; ++k) sum += blo(xr[i][k]) + bhi(xr[i][k]);
        const float mu = rowsum16(sum) * (1.f / DM);
        float q = 0.f;
#pragma unroll
        for (int i = 0; i < 5; ++i)
#pragma unroll
            for (int k = 0; k < 4; ++k) { float a = blo(xr[i][k]) - mu, b = bhi(xr[i][k]) - mu; if (i < 4 || j < 8) q += a * a + b * b; }
        const float rstd = 1.f / sqrtf(rowsum16(q) * (1.f / DM) + 1e-5f);
        if (!have_mod) {
            if (w == 0) wait_flags(P, rf, FL_MOD, 9, l, 0x400u);
            __syncthreads();
            for (int i = t; i < 2 * DM; i += 512) {
                float v = u2f(__builtin_amdgcn_raw_buffer_load_b32(rs, WS_MOD + (unsigned)i * 4u, 0, 16));
                if (i >= DM) *(LAS float*)(L + MOD_OFF + (i - DM) * 4) = rbf(1.f + v); else *(LAS float*)(L + MOD_OFF + (DM + i) * 4) = v;
            }
            have_mod = true;
        }
        __syncthreads();
#pragma unroll
        for (int i = 0; i < 5; ++i) {
            const int c = (i < 4) ? j + 16 * i : 64 + j;
            if (i < 4 || j < 8) {
                f4 g0 = *(LAS f4*)(L + MOD_OFF + c * 32), g1 = *(LAS f4*)(L + MOD_OFF + c * 32 + 16);
                f4 s0 = *(LAS f4*)(L + MOD_OFF + DM * 4 + c * 32), s1 = *(LAS f4*)(L + MOD_OFF + DM * 4 + c * 32 + 16);
                v4u o;
                o[0] = pkbf(fmaf((blo(xr[i][0]) - mu) * rstd, g0[0], s0[0]), fmaf((bhi(xr[i][0]) - mu) * rstd, g0[1], s0[1]));
                o[1] = pkbf(fmaf((blo(xr[i][1]) - mu) * rstd, g0[2], s0[2]), fmaf((bhi(xr[i][1]) - mu) * rstd, g0[3], s0[3]));
                o[2] = pkbf(fmaf((blo(xr[i][2]) - mu) * rstd, g1[0], s1[0]), fmaf((bhi(xr[i][2]) - mu) * rstd, g1[1], s1[1]));
                o[3] = pkbf(fmaf((blo(xr[i][3]) - mu) * rstd, g1[2], s1[2]), fmaf((bhi(xr[i][3]) - mu) * rstd, g1[3], s1[3]));
                *(LAS v4u*)(L + AT_OFF + (tt * 18 + (c >> 2)) * 1024 + (c & 3) * 256 + ((n ^ (c & 15)) * 16)) = o;
            }
        }
        __syncthreads();
#pragma unroll
        for (int i = 0; i < 5; ++i) {
            const int f = w + 8 * i;
            if (f < 36) {
                const int ks = f % 18, g = l >> 4;
                v4u o = *(LAS v4u*)(L + AT_OFF + f * 1024 + g * 256 + ((j ^ ((4 * ks + g) & 15)) * 16));
                stS(o, rs, WS_HF + (unsigned)f * 1024u + l * 16, (unsigned)s * TF_STEP);
            }
        }
        VMWAIT0();
        __syncthreads();
        if (t == 0) set_flag(rf, FL_H + s * 128);
    }
}

__device__ __forceinline__ void p_body(const Params& P, int hq, int rep, int nrep, ldsp L) {
    const int t = threadIdx.x, w = __builtin_amdgcn_readfirstlane(t >> 6), l = t & 63, n = l & 15, g = l >> 4;
    const rsrc_t rs = mkrs(P.ws, WS_END), rf = mkrs(P.flags, FLAG_BYTES);
    const int h = hq * 8 + w;
    const float sc2 = 0.08838834764831845f * 1.4426950408889634f;
    const float NINF = -__builtin_inff();
    for (int s = rep; s < NSEG; s += nrep) {
        wait_flags(P, rf, FL_QK + s * 128 + h * 4, 1, l, 0x800u);
        v4u Q[2][4], K[2][4];
        const unsigned qo = (unsigned)(s * NH + h) * 8192u + l * 16;
#pragma unroll
        for (int tt = 0; tt < 2; ++tt)
#pragma unroll
            for (int u = 0; u < 4; ++u) { Q[tt][u] = ldS(rs, WS_QF + qo + (tt * 4 + u) * 1024u, 0); K[tt][u] = ldS(rs, WS_KF + qo + (tt * 4 + u) * 1024u, 0); }
        f4 z = {0.f, 0.f, 0.f, 0.f};
        f4 s00 = z, s01 = z, s11 = z;
#pragma unroll
        for (int u = 0; u < 4; ++u) { s00 = mf32(K[0][u], Q[0][u], s00); s01 = mf32(K[0][u], Q[1][u], s01); s11 = mf32(K[1][u], Q[1][u], s11); }
#pragma unroll
        for (int r = 0; r < 4; ++r) { if (4 * g + r > n) { s00[r] = NINF; s11[r] = NINF; } }
        wait_flags(P, rf, FL_KM + s * 128 + h * 4, 1, l, 0x800u);
        f4 m0 = z, m1 = z;
#pragma unroll
        for (int u = 0; u < 4; ++u) {
            v4u km = ldS(rs, WS_KM + (unsigned)(s * NH + h) * 2048u + u * 512u + (unsigned)(g * 8 + (n & 7)) * 16u, 0);
            m0 = mf32(km, Q[0][u], m0); m1 = mf32(km, Q[1][u], m1);
        }
        if (g >= 2) { m0 = (f4){NINF, NINF, NINF, NINF}; m1 = m0; }
        const unsigned po = WS_PF + (unsigned)s * PF_STEP;
#pragma unroll
        for (int tt = 0; tt < 2; ++tt) {
            f4 a = tt == 0 ? s00 : s01, b = tt == 0 ? (f4){NINF, NINF, NINF, NINF} : s11, m = tt == 0 ? m0 : m1;
            float mx = max3f(max3f(a[0], a[1], a[2]), max3f(a[3], b[0], b[1]), max3f(b[2], b[3], m[0]));
            mx = max3f(mx, m[1], max2f(m[2], m[3]));
            mx = xmax16(mx); mx = xmax32(mx);
            const float mb = -mx * sc2;
            float ea[4], eb[4], em[4];
#pragma unroll
            for (int q = 0; q < 4; ++q) { ea[q] = __builtin_amdgcn_exp2f(fmaf(a[q], sc2, mb)); eb[q] = __builtin_amdgcn_exp2f(fmaf(b[q], sc2, mb)); em[q] = __builtin_amdgcn_exp2f(fmaf(m[q], sc2, mb)); }
            float sum = ((ea[0] + ea[1]) + (ea[2] + ea[3])) + ((eb[0] + eb[1]) + (eb[2] + eb[3])) + ((em[0] + em[1]) + (em[2] + em[3]));
            sum = xadd16(sum); sum = xadd32(sum);
            const float ri = 1.f / sum;
            v4u pv = {pkbf(ea[0] * ri, ea[1] * ri), pkbf(ea[2] * ri, ea[3] * ri), pkbf(eb[0] * ri, eb[1] * ri), pkbf(eb[2] * ri, eb[3] * ri)};
            stS(pv, rs, po + (unsigned)(tt * 32 + h) * 1024u + l * 16, 0);
            v2u mv = {pkbf(em[0] * ri, em[1] * ri), pkbf(em[2] * ri, em[3] * ri)};
            if (g < 2) __builtin_amdgcn_raw_buffer_store_b64(mv, rs, po + 65536u + (unsigned)(tt * 8 + (h >> 2)) * 1024u + (unsigned)((h & 3) * 16 + n) * 16u + g * 8, 0, 16);
        }
        VMWAIT0();
        if (l == 0) set_flag(rf, FL_P + s * 128 + h * 4);
    }
}

__device__ __forceinline__ void pu_body(const Params& P, int q, int rep, int nrep, ldsp L) {
    const int t = threadIdx.x, w = __builtin_amdgcn_readfirstlane(t >> 6), l = t & 63, n = l & 15, g = l >> 4;
    const rsrc_t rs = mkrs(P.ws, WS_END), rf = mkrs(P.flags, FLAG_BYTES);
    const int tt_e = (w >> 1) & 1, k2 = w & 1;
    const int col0 = 64 * q + 32 * k2 + 8 * g;
    float ga[8];
    if (w == 0) wait_flags(P, rf, FL_MOD + 9 * 4, 5, l, 0x400u);
    __syncthreads();
#pragma unroll
    for (int i = 0; i < 8; ++i) ga[i] = u2f(__builtin_amdgcn_raw_buffer_load_b32(rs, WS_MOD + (unsigned)(2 * DM + col0 + i) * 4u, 0, 16));
    for (int s = rep; s < NSEG; s += nrep) {
        wait_flags(P, rf, FL_VU + s * 128 + w * 16, 4, l, 0x1000u);
        v4u US[4][4];
#pragma unroll
        for (int i = 0; i < 4; ++i)
#pragma unroll
            for (int k = 0; k < 4; ++k) US[i][k] = ldS(rs, WS_US + (unsigned)(4 * q + i) * 32768u + (unsigned)(4 * w + k) * 1024u + l * 16, (unsigned)s * USEG_STEP);
        v4u xin = {0u, 0u, 0u, 0u};
        if (w < 4) xin = *(const v4u*)(P.x + (size_t)(s * 32 + tt_e * 16 + n) * DM + col0);
        wait_flags(P, rf, FL_P + s * 128 + w * 16, 4, l, 0x1000u);
        v4u PS[2][4], PM[2];
        const unsigned po = WS_PF + (unsigned)s * PF_STEP + l * 16;
#pragma unroll
        for (int tt = 0; tt < 2; ++tt) {
#pragma unroll
            for (int k = 0; k < 4; ++k) PS[tt][k] = ldS(rs, po + (unsigned)(tt * 32 + 4 * w + k) * 1024u, 0);
            PM[tt] = ldS(rs, po + 65536u + (unsigned)(tt * 8 + w) * 1024u, 0);
        }
        f4 c[4][2];
#pragma unroll
        for (int i = 0; i < 4; ++i)
#pragma unroll
            for (int tt = 0; tt < 2; ++tt) {
                c[i][tt] = (f4){0.f, 0.f, 0.f, 0.f};
#pragma unroll
                for (int k = 0; k < 4; ++k) c[i][tt] = mf32(US[i][k], PS[tt][k], c[i][tt]);
            }
        wait_flags(P, rf, FL_UM + s * 128 + w * 16, 4, l, 0x1000u);
#pragma unroll
        for (int i = 0; i < 4; ++i) {
            v4u um = ldS(rs, WS_UM + (unsigned)(4 * q + i) * 8192u + (unsigned)w * 1024u + l * 16, (unsigned)s * UMP_STEP);
            c[i][0] = mf32(um, PM[0], c[i][0]); c[i][1] = mf32(um, PM[1], c[i][1]);
        }
#pragma unroll
        for (int i = 0; i < 4; ++i)
#pragma unroll
            for (int tt = 0; tt < 2; ++tt) *(LAS f4*)(L + BR_OFF + w * 8192 + (tt * 4 + i) * 1024 + l * 16) = c[i][tt];
        __syncthreads();
        if (w < 4) {
            f4 a0 = {0.f, 0.f, 0.f, 0.f}, a1 = a0;
            const ldsp rp = L + BR_OFF + (tt_e * 4 + 2 * k2 + (g >> 1)) * 1024 + (n + 32 * (g & 1)) * 16;
#pragma unroll
            for (int k = 0; k < 8; ++k) { a0 += *(LAS f4*)(rp + k * 8192); a1 += *(LAS f4*)(rp + k * 8192 + 256); }
            float y[8];
#pragma unroll
            for (int i = 0; i < 4; ++i) {
                float x0 = (i & 1) ? bhi(xin[i >> 1]) : blo(xin[i >> 1]), x1 = (i & 1) ? bhi(xin[2 + (i >> 1)]) : blo(xin[2 + (i >> 1)]);
                y[i] = rbf(fmaf(ga[i], a0[i], x0)); y[4 + i] = rbf(fmaf(ga[4 + i], a1[i], x1));
            }
            v4u o = {pkbf(y[0], y[1]), pkbf(y[2], y[3]), pkbf(y[4], y[5]), pkbf(y[6], y[7])};
            stS(o, rs, WS_X1 + (unsigned)(tt_e * 18 + 2 * q + k2) * 1024u + l * 16, (unsigned)s * TF_STEP);
            float sm = ((y[0] + y[1]) + (y[2] + y[3])) + ((y[4] + y[5]) + (y[6] + y[7]));
            sm = xadd16(sm); sm = xadd32(sm);
            const float mu = sm * (1.f / 32.f);
            float m2 = 0.f;
#pragma unroll
            for (int i = 0; i < 8; ++i) { float d = y[i] - mu; m2 = fmaf(d, d, m2); }
            m2 = xadd16(m2); m2 = xadd32(m2);
            v2u st = {f2u(mu), f2u(m2)};
            if (g == 0) __builtin_amdgcn_raw_buffer_store_b64(st, rs, WS_ST + (unsigned)(tt_e * 16 + n) * 256u + (unsigned)(2 * q + k2) * 8u, (unsigned)s * ST_STEP, 16);
            VMWAIT0();
        }
        __syncthreads();
        if (t == 0) set_flag(rf, FL_X1 + s * 128 + q * 4);
    }
}

__device__ __forceinline__ float gelu_tanh(float x) {
    const float u = 0.7978845608028654f * (x + 0.044715f * x * x * x);
    const float e = __builtin_amdgcn_exp2f(-2.885390081777927f * u);
    return x * __builtin_amdgcn_rcpf(1.f + e);
}
__device__ __forceinline__ void c1_body(const Params& P, int j, ldsp L) {
    const int t = threadIdx.x, w = __builtin_amdgcn_readfirstlane(t >> 6), l = t & 63, n = l & 15, g = l >> 4;
    const rsrc_t rs = mkrs(P.ws, WS_END), rf = mkrs(P.flags, FLAG_BYTES);
    v4u W[2][18];
#pragma unroll
    for (int m = 0; m < 2; ++m) {
        stage_w128(m == 0 ? P.Wg : P.Wu, FF, j * 128, L, t);
#pragma unroll
        for (int ks = 0; ks < 18; ++ks) W[m][ks] = dfrag(L, 256, ks * 32, (w >> 1) * 64 + (w & 1) * 8, 16, l, 7);
    }
    if (w == 0) wait_flags(P, rf, FL_MOD + 13 * 4, 10, l, 0x400u);
    __syncthreads();
    for (int i = t; i < DM; i += 512) {
        float sb = u2f(__builtin_amdgcn_raw_buffer_load_b32(rs, WS_MOD + (unsigned)(3 * DM + i) * 4u, 0, 16));
        float cb = u2f(__builtin_amdgcn_raw_buffer_load_b32(rs, WS_MOD + (unsigned)(4 * DM + i) * 4u, 0, 16));
        *(LAS u16*)(L + i * 2) = (u16)(f2u(sb) >> 16);
        *(LAS float*)(L + 2048 + i * 4) = rbf(1.f + cb);
    }
    __syncthreads();
    f4 bias[2], cs[2];
#pragma unroll
    for (int m = 0; m < 2; ++m) { bias[m] = (f4){0.f, 0.f, 0.f, 0.f}; cs[m] = bias[m]; }
    const v4u onesb = {0x3F803F80u, 0x3F803F80u, 0x3F803F80u, 0x3F803F80u};
#pragma unroll
    for (int ks = 0; ks < 18; ++ks) {
        const v4u sbf = *(LAS v4u*)(L + (ks * 32 + 8 * g) * 2);
        const f4 g0 = *(LAS f4*)(L + 2048 + (ks * 32 + 8 * g) * 4), g1 = *(LAS f4*)(L + 2048 + (ks * 32 + 8 * g) * 4 + 16);
#pragma unroll
        for (int m = 0; m < 2; ++m) {
            bias[m] = mf32(W[m][ks], sbf, bias[m]);
            v4u x = W[m][ks];
            v4u y = {pkbf(blo(x[0]) * g0[0], bhi(x[0]) * g0[1]), pkbf(blo(x[1]) * g0[2], bhi(x[1]) * g0[3]), pkbf(blo(x[2]) * g1[0], bhi(x[2]) * g1[1]), pkbf(blo(x[3]) * g1[2], bhi(x[3]) * g1[3])};
            asm volatile("" : "+v"(y));
            W[m][ks] = y;
            cs[m] = mf32(y, onesb, cs[m]);
        }
    }
    __syncthreads();
    for (int s = 0; s < NSEG; ++s) {
        if (w == 0) wait_flags(P, rf, FL_X1 + s * 128, 9, l, 0x2000u);
        __syncthreads();
        v4u T[5];
        TILE_LOAD(T, WS_X1 + (unsigned)s * TF_STEP);
        float mu[2], rs_[2];
        {
            v2u st[2][5];
#pragma unroll
            for (int tt = 0; tt < 2; ++tt)
#pragma unroll
                for (int i = 0; i < 5; ++i) {
                    const int slot = g + 4 * i;
                    st[tt][i] = __builtin_amdgcn_raw_buffer_load_b64(rs, WS_ST + (unsigned)(tt * 16 + n) * 256u + (unsigned)(slot < 18 ? slot : 0) * 8u, (unsigned)s * ST_STEP, 16);
                }
            TILE_STORE(T);
#pragma unroll
            for (int tt = 0; tt < 2; ++tt) {
                float sm = 0.f;
#pragma unroll
                for (int i = 0; i < 5; ++i) sm += (g + 4 * i < 18) ? u2f(st[tt][i][0]) : 0.f;
                sm = xadd16(sm); sm = xadd32(sm);
                const float m = sm * (1.f / 18.f);
                float q = 0.f;
#pragma unroll
                for (int i = 0; i < 5; ++i) { float d = u2f(st[tt][i][0]) - m; q += (g + 4 * i < 18) ? fmaf(32.f * d, d, u2f(st[tt][i][1])) : 0.f; }
                q = xadd16(q); q = xadd32(q);
                mu[tt] = m; rs_[tt] = 1.f / sqrtf(q * (1.f / DM) + 1e-5f);
            }
        }
        __syncthreads();
        f4 c[2][2];
#pragma unroll
        for (int m = 0; m < 2; ++m) { c[m][0] = (f4){0.f, 0.f, 0.f, 0.f}; c[m][1] = c[m][0]; }
#pragma unroll
        for (int ks = 0; ks < 18; ++ks) {
            v4u b0 = *(LAS v4u*)(L + AT_OFF + ks * 1024 + l * 16), b1 = *(LAS v4u*)(L + AT_OFF + (18 + ks) * 1024 + l * 16);
            c[0][0] = mf32(W[0][ks], b0, c[0][0]); c[1][0] = mf32(W[1][ks], b0, c[1][0]);
            c[0][1] = mf32(W[0][ks], b1, c[0][1]); c[1][1] = mf32(W[1][ks], b1, c[1][1]);
        }
#pragma unroll
        for (int tt = 0; tt < 2; ++tt) {
            float a[4];
#pragma unroll
            for (int r = 0; r < 4; ++r) {
                const float gv = fmaf(rs_[tt], fmaf(-mu[tt], cs[0][r], c[0][tt][r]), bias[0][r]);
                const float uv = fmaf(rs_[tt], fmaf(-mu[tt], cs[1][r], c[1][tt][r]), bias[1][r]);
                a[r] = gelu_tanh(gv) * uv;
            }
            *(LAS v2u*)(L + XC_OFF + tt * 4096 + (w >> 1) * 1024 + l * 16 + (w & 1) * 8) = (v2u){pkbf(a[0], a[1]), pkbf(a[2], a[3])};
        }
        __syncthreads();
        { v4u o = *(LAS v4u*)(L + XC_OFF + t * 16);
          stS(o, rs, WS_ACT + (unsigned)((w >> 2) * 144 + 4 * j + (w & 3)) * 1024u + l * 16, (unsigned)s * ACT_STEP); }
        VMWAIT0();
        __syncthreads();
        if (t == 0) set_flag(rf, FL_ACT + s * 256 + j * 4);
    }
}

__device__ __forceinline__ void c2_body(const Params& P, int cidx, int rep, int nrep, ldsp L) {
    const int t = threadIdx.x, w = __builtin_amdgcn_readfirstlane(t >> 6), l = t & 63, n = l & 15, g = l >> 4;
    const rsrc_t rs = mkrs(P.ws, WS_END), rf = mkrs(P.flags, FLAG_BYTES);
    v4u W[2][18];
    {
        const ldsp S = L + w * 15360;
#pragma unroll
        for (int pth = 0; pth < 3; ++pth) {
            v4u tmp[12];
#pragma unroll
            for (int i = 0; i < 12; ++i) { const int ch = l + 64 * i; tmp[i] = *(const v4u*)(P.Wd + (size_t)(w * DM + pth * 192 + (ch >> 2)) * DM + cidx * 32 + (ch & 3) * 8); }
            asm volatile("s_waitcnt lgkmcnt(0)" ::: "memory");
#pragma unroll
            for (int i = 0; i < 12; ++i) { const int ch = l + 64 * i; *(LAS v4u*)(S + (ch >> 2) * 80 + (ch & 3) * 16) = tmp[i]; }
            asm volatile("s_waitcnt lgkmcnt(0)" ::: "memory");
#pragma unroll
            for (int k = 0; k < 6; ++k) { W[0][pth * 6 + k] = dfrag(S, 80, k * 32, 0, 16, l, 0); W[1][pth * 6 + k] = dfrag(S, 80, k * 32, 8, 16, l, 0); }
        }
    }
    float gb[8];
    if (w == 0) wait_flags(P, rf, FL_MOD + 22 * 4, 5, l, 0x400u);
    __syncthreads();
#pragma unroll
    for (int i = 0; i < 8; ++i) gb[i] = u2f(__builtin_amdgcn_raw_buffer_load_b32(rs, WS_MOD + (unsigned)(5 * DM + cidx * 32 + 8 * g + i) * 4u, 0, 16));
    for (int s = rep; s < NSEG; s += nrep) {
        const int j0 = (DM * w) >> 7;
        wait_flags(P, rf, FL_ACT + s * 256 + j0 * 4, (w & 1) ? 5 : 5, l, 0x4000u);
        f4 c[2][2];
#pragma unroll
        for (int a = 0; a < 2; ++a) { c[a][0] = (f4){0.f, 0.f, 0.f, 0.f}; c[a][1] = c[a][0]; }
        const unsigned ao = WS_ACT + (unsigned)(18 * w) * 1024u + l * 16;
#pragma unroll
        for (int hb = 0; hb < 3; ++hb) {
            v4u B[2][6];
#pragma unroll
            for (int k = 0; k < 6; ++k) { B[0][k] = ldS(rs, ao + (unsigned)(hb * 6 + k) * 1024u, (unsigned)s * ACT_STEP); B[1][k] = ldS(rs, ao + (unsigned)(144 + hb * 6 + k) * 1024u, (unsigned)s * ACT_STEP); }
#pragma unroll
            for (int k = 0; k < 6; ++k) {
                c[0][0] = mf32(W[0][hb * 6 + k], B[0][k], c[0][0]); c[1][0] = mf32(W[1][hb * 6 + k], B[0][k], c[1][0]);
                c[0][1] = mf32(W[0][hb * 6 + k], B[1][k], c[0][1]); c[1][1] = mf32(W[1][hb * 6 + k], B[1][k], c[1][1]);
            }
        }
        v4u xin = {0u, 0u, 0u, 0u};
        if (w < 2) xin = ldS(rs, WS_X1 + (unsigned)(w * 18 + cidx) * 1024u + l * 16, (unsigned)s * TF_STEP);
        __syncthreads();
#pragma unroll
        for (int tt = 0; tt < 2; ++tt)
#pragma unroll
            for (int a = 0; a < 2; ++a) *(LAS f4*)(L + w * 4096 + (tt * 2 + a) * 1024 + l * 16) = c[a][tt];
        __syncthreads();
        if (w < 2) {
            f4 a0 = {0.f, 0.f, 0.f, 0.f}, a1 = a0;
#pragma unroll
            for (int k = 0; k < 8; ++k) { a0 += *(LAS f4*)(L + k * 4096 + (w * 2) * 1024 + l * 16); a1 += *(LAS f4*)(L + k * 4096 + (w * 2 + 1) * 1024 + l * 16); }
            v4u o;
            o[0] = pkbf(fmaf(gb[0], a0[0], blo(xin[0])), fmaf(gb[1], a0[1], bhi(xin[0])));
            o[1] = pkbf(fmaf(gb[2], a0[2], blo(xin[1])), fmaf(gb[3], a0[3], bhi(xin[1])));
            o[2] = pkbf(fmaf(gb[4], a1[0], blo(xin[2])), fmaf(gb[5], a1[1], bhi(xin[2])));
            o[3] = pkbf(fmaf(gb[6], a1[2], blo(xin[3])), fmaf(gb[7], a1[3], bhi(xin[3])));
            *(v4u*)(P.out + (size_t)(s * 32 + w * 16 + n) * DM + cidx * 32 + 8 * g) = o;
        }
    }
}

__global__ void __launch_bounds__(512, 2) fused_kernel(Params P) {
    extern __shared__ __attribute__((aligned(16))) unsigned char lds[];
    const ldsp L = (ldsp)lds;
    if (threadIdx.x == 0) {
        GAS unsigned* f = (GAS unsigned*)P.flags;
        const unsigned xcc = __builtin_amdgcn_s_getreg((31 << 11) | 20) & 0xfu;
        int role = -1;
        if (xcc < 4u) { unsigned tk = __hip_atomic_fetch_add(f + FL_TA / 4, 1u, __ATOMIC_RELAXED, __HIP_MEMORY_SCOPE_AGENT); if (tk < 68u) role = (int)tk; }
        if (role < 0) { unsigned tk = __hip_atomic_fetch_add(f + FL_TB / 4, 1u, __ATOMIC_RELAXED, __HIP_MEMORY_SCOPE_AGENT); role = tk < 188u ? 68 + (int)tk : 67 - ((int)tk - 188); }
        *(LAS int*)(L + LDS_BYTES - 32) = role;
    }
    __syncthreads();
    const int role = __builtin_amdgcn_readfirstlane(*(LAS int*)(L + LDS_BYTES - 32));
    if (role < 36) a_body(P, role, L);
    else if (role < 68) b_body(P, role - 36, L);
    else {
        const int b = role - 68;
        if (b < 26) { mod_slice(P, b, L); __syncthreads(); ln1_body(P, b, 26, L); }
        else if (b < 58) qk_body(P, b - 26, L);
        else if (b < 90) vu_body(P, b - 58, L);
        else if (b < 126) c1_body(P, b - 90, L);
        else if (b < 162) c2_body(P, (b - 126) % 18, (b - 126) / 18, 2, L);
        else if (b < 180) { if (b == 162) { mod_slice(P, 26, L); __syncthreads(); } pu_body(P, (b - 162) % 9, (b - 162) / 9, 2, L); }
        else p_body(P, (b - 180) & 3, (b - 180) >> 2, 2, L);
    }
}

extern "C" void kernel_launch(void* const* d_in, const int* in_sizes, int n_in, void* d_out, int out_size, void* d_ws, size_t ws_size, hipStream_t stream) {
    Params P;
    P.x = (const u16*)d_in[0]; P.mem0 = (const u16*)d_in[1]; P.Wq = (const u16*)d_in[2]; P.Wk = (const u16*)d_in[3]; P.Wv = (const u16*)d_in[4]; P.Wo = (const u16*)d_in[5];
    P.Wg = (const u16*)d_in[8]; P.Wu = (const u16*)d_in[9]; P.Wd = (const u16*)d_in[10]; P.c = (const u16*)d_in[11]; P.wmod = (const u16*)d_in[12];
    P.out = (u16*)d_out; P.flags = (unsigned*)d_ws;
    P.ws = (unsigned char*)(((size_t)d_ws + FLAG_BYTES + (2u << 20) - 1) & ~(size_t)((2u << 20) - 1));
    (void)hipFuncSetAttribute((const void*)fused_kernel, hipFuncAttributeMaxDynamicSharedMemorySize, LDS_BYTES);
    (void)hipMemsetAsync(d_ws, 0, FLAG_BYTES, stream);
    fused_kernel<<<256, 512, LDS_BYTES, stream>>>(P);
}
```

```cpp
#include <hip/hip_runtime.h>
#include <hip/hip_bf16.h>

typedef unsigned short u16;
typedef short s4 __attribute__((ext_vector_type(4)));
typedef __bf16 bf8 __attribute__((ext_vector_type(8)));
typedef __bf16 bf16v __attribute__((ext_vector_type(16)));
typedef float f4 __attribute__((ext_vector_type(4)));
typedef float f2 __attribute__((ext_vector_type(2)));
typedef unsigned v2u __attribute__((ext_vector_type(2)));
typedef unsigned v4u __attribute__((ext_vector_type(4)));
typedef unsigned v8u __attribute__((ext_vector_type(8)));
#define LAS __attribute__((address_space(3)))
#define GAS __attribute__((address_space(1)))
typedef LAS unsigned char* ldsp;
typedef __amdgpu_buffer_rsrc_t rsrc_t;

constexpr int DM = 576, NH = 32, EH = 128, NSEG = 64, HE = NH * EH, FF = 4608;
constexpr int LDS_BYTES = 147456 + 8192;

constexpr unsigned FL_ERR = 0, FL_TA = 128, FL_TB = 256, FL_PAR = 384, FL_INIT = 512, FL_MOD = 1024;
constexpr unsigned FL_H = 1536;
constexpr unsigned FL_TX = 2048;
constexpr unsigned FL_QK = 4096, FL_VU = 12288, FL_KM = 20480, FL_UM = 28672, FL_X1 = 36864  , FL_P = 53248  , FL_ACT = 69632  ;
constexpr unsigned FLAG_BYTES = 69632 + 64 * 1280;
constexpr unsigned UM_SLOT = 36u * 16384u;
constexpr unsigned PB_BASE = 1728u << 10, PB_SLOT = 49152u;
constexpr unsigned MEM_BASE = 1872u << 10, MEM_SLOT = 32768u;
constexpr unsigned PH_BASE = 1968u << 10;
constexpr unsigned CAL_BASE = 1984u << 10;
constexpr unsigned WS_QF = 2u << 20, WS_KF = 18u << 20;
constexpr unsigned WS_US = 34u << 20, USEG_STEP = 36u * 32768u;
constexpr unsigned WS_UM = 106u << 20, UMP_STEP = 36u * 8192u;
constexpr unsigned WS_KM = 124u << 20;
constexpr unsigned WS_PF = 128u << 20, PF_STEP = 81920u;
constexpr unsigned WS_HF = 134u << 20, TF_STEP = 36864u;
constexpr unsigned WS_X1 = 137u << 20;
constexpr unsigned WS_ST = 140u << 20, ST_STEP = 8192u;
constexpr unsigned WS_MOD = 141u << 20;
constexpr unsigned WS_ACT = 142u << 20, ACT_STEP = 294912u;
constexpr unsigned WS_END = 161u << 20;
constexpr unsigned SENT = 0xFFFFFFFFu;
constexpr int SPIN_MAX = 100000;

struct Params {
    const u16 *x, *mem0, *Wq, *Wk, *Wv, *Wo, *Wg, *Wu, *Wd, *c, *wmod;
    u16* out; unsigned* flags; unsigned char* ws;
};

__device__ __forceinline__ v2u trread(ldsp p) { s4 r = __builtin_amdgcn_ds_read_tr16_b64_v4i16((LAS s4*)p); return __builtin_bit_cast(v2u, r); }
__device__ __forceinline__ unsigned pkbf(float lo, float hi) {
    typedef __bf16 bf2 __attribute__((ext_vector_type(2)));
    bf2 r; r[0] = (__bf16)lo; r[1] = (__bf16)hi; return __builtin_bit_cast(unsigned, r); }
__device__ __forceinline__ f4 smf(v4u a, v8u b, f4 c, int idx) { return __builtin_amdgcn_smfmac_f32_16x16x64_bf16(__builtin_bit_cast(bf8, a), __builtin_bit_cast(bf16v, b), c, idx, 0, 0); }
__device__ __forceinline__ f4 mf32(v4u a, v4u b, f4 c) { return __builtin_amdgcn_mfma_f32_16x16x32_bf16(__builtin_bit_cast(bf8, a), __builtin_bit_cast(bf8, b), c, 0, 0, 0); }
__device__ __forceinline__ float u2f(unsigned x) { return __builtin_bit_cast(float, x); }
__device__ __forceinline__ unsigned f2u(float x) { return __builtin_bit_cast(unsigned, x); }
__device__ __forceinline__ float blo(unsigned x) { return u2f(x << 16); }
__device__ __forceinline__ float bhi(unsigned x) { return u2f(x & 0xFFFF0000u); }
__device__ __forceinline__ float rbf(float x) { return blo(pkbf(x, 0.f) & 0xFFFFu); }
__device__ __forceinline__ float swadd32(float a, float b) { auto r = __builtin_amdgcn_permlane32_swap(f2u(a), f2u(b), false, false); return u2f(r[0]) + u2f(r[1]); }
__device__ __forceinline__ float max3f(float a, float b, float c) { float r; asm("v_max3_f32 %0, %1, %2, %3" : "=v"(r) : "v"(a), "v"(b), "v"(c)); return r; }
__device__ __forceinline__ float max2f(float a, float b) { float r; asm("v_max_f32 %0, %1, %2" : "=v"(r) : "v"(a), "v"(b)); return r; }
__device__ __forceinline__ float xmax16(float a) { auto r = __builtin_amdgcn_permlane16_swap(f2u(a), f2u(a), false, false); return max2f(u2f(r[0]), u2f(r[1])); }
__device__ __forceinline__ float xmax32(float a) { auto r = __builtin_amdgcn_permlane32_swap(f2u(a), f2u(a), false, false); return max2f(u2f(r[0]), u2f(r[1])); }
__device__ __forceinline__ float xadd16(float a) { auto r = __builtin_amdgcn_permlane16_swap(f2u(a), f2u(a), false, false); return u2f(r[0]) + u2f(r[1]); }
__device__ __forceinline__ float xadd32(float a) { auto r = __builtin_amdgcn_permlane32_swap(f2u(a), f2u(a), false, false); return u2f(r[0]) + u2f(r[1]); }
template <int CTRL> __device__ __forceinline__ float dppf(float v) { return u2f((unsigned)__builtin_amdgcn_update_dpp(0, (int)f2u(v), CTRL, 0xf, 0xf, false)); }
__device__ __forceinline__ float rowsum16(float v) { v += dppf<0x121>(v); v += dppf<0x122>(v); v += dppf<0x124>(v); v += dppf<0x128>(v); return v; }
__device__ __forceinline__ unsigned umax4(v4u x) { return max(max(x[0], x[1]), max(x[2], x[3])); }
__device__ __forceinline__ v8u cat8(v4u a, v4u b) { return (v8u){a[0], a[1], a[2], a[3], b[0], b[1], b[2], b[3]}; }
__device__ __forceinline__ float maxc(float a, float b) { return __builtin_amdgcn_fmed3f(a, b, __builtin_inff()); }
__device__ __forceinline__ void rdelay(int n) {
#pragma nounroll
    for (int i = 0; i < n; ++i) asm volatile("s_nop 0");
}
__device__ __forceinline__ rsrc_t mkrs(const void* p, unsigned bytes) { return __builtin_amdgcn_make_buffer_rsrc((void*)p, 0, bytes, 0x00020000); }
__device__ __forceinline__ v4u ldS(rsrc_t r, unsigned off, unsigned so) { return __builtin_amdgcn_raw_buffer_load_b128(r, off, so, 16); }
__device__ __forceinline__ void stS(v4u v, rsrc_t r, unsigned off, unsigned so) { __builtin_amdgcn_raw_buffer_store_b128(v, r, off, so, 16); }
#define WGBAR() do { asm volatile("s_waitcnt lgkmcnt(0)" ::: "memory"); __builtin_amdgcn_s_barrier(); asm volatile("" ::: "memory"); } while (0)
#define LD16(dst, rsrc, off, so) asm volatile("buffer_load_dwordx4 %0, %1, %2, %3 offen sc1" : "=v"(dst) : "v"(off), "s"(rsrc), "s"(so) : "memory")
#define LD16I(dst, rsrc, off, so, imm) asm volatile("buffer_load_dwordx4 %0, %1, %2, %3 offen offset:" #imm " sc1" : "=v"(dst) : "v"(off), "s"(rsrc), "s"(so) : "memory")
#define LD4(dst, rsrc, off, so) asm volatile("buffer_load_dword %0, %1, %2, %3 offen sc1" : "=v"(dst) : "v"(off), "s"(rsrc), "s"(so) : "memory")
#define ST16S(val, rsrc, off, so) asm volatile("buffer_store_dwordx4 %0, %1, %2, %3 offen sc1\n\ts_nop 1" :: "v"(val), "v"(off), "s"(rsrc), "s"(so) : "memory")
#define ST8S(val, rsrc, off, so) asm volatile("buffer_store_dwordx2 %0, %1, %2, %3 offen sc1\n\ts_nop 1" :: "v"(val), "v"(off), "s"(rsrc), "s"(so) : "memory")
#define ST4S(val, rsrc, off, so) asm volatile("buffer_store_dword %0, %1, %2, %3 offen sc1" :: "v"(val), "v"(off), "s"(rsrc), "s"(so) : "memory")
#define VMWAIT0() asm volatile("s_waitcnt vmcnt(0)" ::: "memory")

#define TS(slot, s)
#define DBGM(slot, v)
#define EV(id, idx)
__device__ __forceinline__ void set_err(const Params& P, unsigned code) { __hip_atomic_fetch_or((GAS unsigned*)P.flags, code, __ATOMIC_RELAXED, __HIP_MEMORY_SCOPE_AGENT); }
__device__ __forceinline__ bool spin_fail(const Params& P, int sp, int l, unsigned code) {
    if ((sp & 1023) != 1023) return false;
    if (__builtin_amdgcn_readfirstlane(__hip_atomic_load((GAS unsigned*)P.flags, __ATOMIC_RELAXED, __HIP_MEMORY_SCOPE_AGENT)) != 0u) return true;
    if (sp > SPIN_MAX) { if (l == 0) set_err(P, code); return true; }
    return false;
}
__device__ __forceinline__ void wait_flags(const Params& P, rsrc_t rf, unsigned off, int cnt, int l, unsigned code) {
    const unsigned o = off + (unsigned)(l < cnt ? l : 0) * 4u;
    for (int sp = 0;; ++sp) {
        unsigned fl; LD4(fl, rf, o, 0u);
        asm volatile("s_waitcnt vmcnt(0)" : "+v"(fl) :: "memory");
        if (__all(fl != 0u)) break;
        if (spin_fail(P, sp, l, code)) break;
        __builtin_amdgcn_s_sleep(2);
    }
}
__device__ __forceinline__ void set_flag(rsrc_t rf, unsigned off) { unsigned one = 1u; ST4S(one, rf, off, 0u); }

constexpr int MEMT_OFF = 0;
constexpr int T_OFF = MEMT_OFF + 11520;
constexpr int KS_OFF = T_OFF + 6144;
constexpr int RED_OFF = 0;
constexpr int NREC = 100;
constexpr int C_U = 110, C_P = 130, T_PER = 200, C_SLIP = 60, C_B = 12;
constexpr int BASE_OFF = 40960;

__device__ __forceinline__ v8u bfrag_sp(ldsp stg, int rowbytes, int row0, int cg, int l, int swzmask) {
    const int gp = l >> 4, q = (l & 15) >> 2, p = l & 3;
    v8u r;
#pragma unroll
    for (int J = 0; J < 2; ++J) {
        const int G = 2 * J + (gp >> 1);
        const int d0 = row0 + 16 * G + 4 * (gp & 1) + q, d1 = d0 + 8;
        v2u x = trread(stg + d0 * rowbytes + ((cg ^ (d0 & swzmask)) * 32) + p * 8);
        v2u y = trread(stg + d1 * rowbytes + ((cg ^ (d1 & swzmask)) * 32) + p * 8);
        r[4 * J + 0] = x[0]; r[4 * J + 1] = y[0]; r[4 * J + 2] = x[1]; r[4 * J + 3] = y[1];
    }
    asm volatile("" : "+v"(r));
    return r;
}
__device__ __forceinline__ void stage_w128(const u16* W, int ld, int col0, ldsp L, int t) {
    v4u tmp[18];
#pragma unroll
    for (int i = 0; i < 18; ++i) { int ch = t + 512 * i; int d = ch >> 4, cc = ch & 15; tmp[i] = *(const v4u*)(W + (size_t)d * ld + col0 + cc * 8); }
    __syncthreads();
#pragma unroll
    for (int i = 0; i < 18; ++i) { int ch = t + 512 * i; int d = ch >> 4, cc = ch & 15; *(LAS v4u*)(L + d * 256 + ((cc ^ ((d & 7) << 1)) * 16)) = tmp[i]; }
    __syncthreads();
}
__device__ __forceinline__ void stage_wo64(const u16* Wo, int row0, ldsp L, int t) {
    v4u tmp[9];
#pragma unroll
    for (int i = 0; i < 9; ++i) { int ch = t + 512 * i; int e = ch / 72, cc = ch % 72; tmp[i] = *(const v4u*)(Wo + (size_t)(row0 + e) * DM + cc * 8); }
    __syncthreads();
#pragma unroll
    for (int i = 0; i < 9; ++i) { int ch = t + 512 * i; int e = ch / 72, cc = ch % 72; *(LAS v4u*)(L + e * 1184 + cc * 16) = tmp[i]; }
    __syncthreads();
}
__device__ __forceinline__ void load_mem0(const u16* mem0, ldsp L, int t) {
    for (int i = t; i < 8 * DM; i += 512) { int r = i / DM, d = i % DM; *(LAS u16*)(L + MEMT_OFF + d * 16 + (d >> 4) * 64 + r * 2) = mem0[i]; }
}
#define MEM_GATHER(EXTRA, errcode) do { \
        v4u x0, x1 = {0u, 0u, 0u, 0u}; \
        if (EXTRA) LD16(x1, rs, mg1, msl); \
        LD16(x0, rs, mg0, msl); \
        asm volatile("s_waitcnt vmcnt(0)" : "+v"(x0), "+v"(x1) :: "memory"); \
        if (__builtin_expect(!__all(max(umax4(x0), umax4(x1)) != SENT), 0)) { \
            for (int sp = 0;; ++sp) { \
                if (EXTRA) LD16(x1, rs, mg1, msl); \
                LD16(x0, rs, mg0, msl); \
                asm volatile("s_waitcnt vmcnt(0)" : "+v"(x0), "+v"(x1) :: "memory"); \
                if (__all(max(umax4(x0), umax4(x1)) != SENT)) break; \
                if (spin_fail(P, sp, l, errcode)) { DBGM(11, __ballot(max(umax4(x0), umax4(x1)) == SENT)); break; } \
            } \
        } \
        *(LAS v4u*)mw0 = x0; if (EXTRA) *(LAS v4u*)mw1 = x1; \
    } while (0)
__device__ __forceinline__ void wait_until(unsigned tgt) {
    for (;;) {
        const int d = (int)(tgt - (unsigned)__builtin_amdgcn_s_memrealtime());
        if (d <= 0 || d > 2000) break;
    }
}

__device__ __forceinline__ unsigned rec_handshake(const Params& P, ldsp L, int t) {
    VMWAIT0();
    __syncthreads();
    if (t < 64) {
        GAS unsigned* f = (GAS unsigned*)P.flags;
        if (t == 0) __hip_atomic_fetch_add(f + FL_INIT / 4, 1u, __ATOMIC_RELAXED, __HIP_MEMORY_SCOPE_AGENT);
        unsigned par = 1u;
        for (int sp = 0;; ++sp) {
            unsigned c = __hip_atomic_load(f + FL_INIT / 4, __ATOMIC_RELAXED, __HIP_MEMORY_SCOPE_AGENT);
            par = __hip_atomic_load(f + FL_PAR / 4, __ATOMIC_RELAXED, __HIP_MEMORY_SCOPE_AGENT);
            if (c >= 36u && par != 0u) break;
            if (sp > SPIN_MAX) { if (t == 0) set_err(P, 0x100u); par = 1u; break; }
            __builtin_amdgcn_s_sleep(2);
        }
        if (t == 0) *(LAS unsigned*)(L + LDS_BYTES - 16) = par - 1u;
    }
    __syncthreads();
    return __builtin_amdgcn_readfirstlane(*(LAS unsigned*)(L + LDS_BYTES - 16));
}
__device__ __forceinline__ unsigned rec_wait_init(const Params& P, ldsp L, int t) {
    if (t < 64) {
        GAS unsigned* f = (GAS unsigned*)P.flags;
        unsigned par = 1u;
        for (int sp = 0;; ++sp) {
            unsigned c = __hip_atomic_load(f + FL_INIT / 4, __ATOMIC_RELAXED, __HIP_MEMORY_SCOPE_AGENT);
            par = __hip_atomic_load(f + FL_PAR / 4, __ATOMIC_RELAXED, __HIP_MEMORY_SCOPE_AGENT);
            if (c >= 36u && par != 0u) break;
            if (sp > SPIN_MAX) { if (t == 0) set_err(P, 0x100u); par = 1u; break; }
            __builtin_amdgcn_s_sleep(2);
        }
        if (t == 0) *(LAS unsigned*)(L + LDS_BYTES - 16) = par - 1u;
    }
    __syncthreads();
    return __builtin_amdgcn_readfirstlane(*(LAS unsigned*)(L + LDS_BYTES - 16));
}

template <int ROLE>
__device__ __forceinline__ void a_loop(const Params& P, int h, ldsp L, const v8u (&WQ)[9], const v8u (&WK)[9], unsigned par) {
    const int t = threadIdx.x, w = __builtin_amdgcn_readfirstlane(t >> 6), l = t & 63, n = l & 15, g = l >> 4;
    const rsrc_t rs = mkrs(P.ws, WS_END), rf = mkrs(P.flags, FLAG_BYTES);
    const unsigned pa = par << 13;
    const int sidx = (n & 8) ? 0xEEEE : 0x4444;
    const int q4 = n >> 2, p4 = n & 3;
    const ldsp memA = L + MEMT_OFF + (16 * g + 8 * (p4 >> 1) + q4) * 16 + g * 64 + (p4 & 1) * 8;
    const ldsp tW = L + T_OFF + (16 * w + n) * 32 + ((16 * w + n) >> 3) * 128 + g * 8;
    const unsigned mg0 = MEM_BASE + t * 16 + pa, mg1 = MEM_BASE + 16384 + (w == 7 ? l * 16 : 1024) + pa;
    const ldsp mw0 = L + MEMT_OFF + t * 16 + (t >> 4) * 64, mw1 = (w == 7) ? L + MEMT_OFF + (512 + l) * 16 + ((512 + l) >> 4) * 64 : L + BASE_OFF;
    auto pmap = [&](unsigned off) { return PB_BASE + ((off >> 13) << 14) + (off & 8191u) + pa; };
    const ldsp tR = L + T_OFF + (8 * g + q4) * 32 + g * 128 + p4 * 8;
    const int i8 = n & 7;
    const unsigned pst0 = pmap((unsigned)(i8 >> 2) * 10240u + (unsigned)(h >> 2) * 1024u + (unsigned)(16 * (2 * (h & 1) + (g >> 1)) + 8 * (g & 1) + 4 * ((h >> 1) & 1) + (i8 & 3)) * 16u);
    const unsigned pst1 = pmap((unsigned)(i8 >> 2) * 10240u + (unsigned)(8 + (h >> 4)) * 1024u + (unsigned)(16 * (2 * ((h >> 2) & 1) + ((h & 3) >> 1)) + 8 * (g & 1) + 4 * ((h >> 3) & 1) + (i8 & 3)) * 16u + 8u * (h & 1));
    const float sc2 = 0.08838834764831845f * 1.4426950408889634f;
    const float NINF = -__builtin_inff();
    const int tt = w - 1;
    for (int s = 0; s < NSEG; ++s) {
        const unsigned psl = (unsigned)(s % 3) * PB_SLOT;
        const ldsp KS = L + KS_OFF + (s & 1) * 8192;
        if (ROLE == 0 && h == 0) TS(0, s);
        if (ROLE == 0) EV(h, 0);
        v4u A[9];
#define RD(kb) do { v2u x_ = trread(memA + (kb) * 1280), y_ = trread(memA + (kb) * 1280 + 64); A[kb] = (v4u){x_[0], x_[1], y_[0], y_[1]}; } while (0)
#define SB() __builtin_amdgcn_sched_barrier(0)
#define MM(kb) do { aq = smf(A[kb], WQ[kb], aq, sidx); ak = smf(A[kb], WK[kb], ak, sidx); } while (0)
        f4 aq = {0.f, 0.f, 0.f, 0.f}, ak = aq;
        v4u x0, x1, x2, x3; unsigned flg = 1u;
        RD(0); RD(1); RD(2); SB();
        MM(0); SB(); RD(3); RD(4); RD(5); SB();
        MM(1); MM(2); SB(); RD(6); RD(7); RD(8); SB();
        MM(3); SB();
        if (ROLE == 1) {
            const unsigned qo = WS_QF + (unsigned)(s * NH + h) * 8192u + (unsigned)tt * 4096u;
            LD16I(x0, rs, l * 16, qo, 0); LD16I(x1, rs, l * 16, qo, 1024); LD16I(x2, rs, l * 16, qo, 2048); LD16I(x3, rs, l * 16, qo, 3072);
        }
        if (ROLE == 2) {
            if (w >= 6) {
                const unsigned ko = WS_KF + (unsigned)((s + 1 < NSEG ? s + 1 : s) * NH + h) * 8192u + (unsigned)(w & 1) * 4096u;
                LD4(flg, rf, FL_QK + h * 4, (unsigned)(s + 2 < NSEG ? s + 2 : 0) * 128u);
                LD16I(x0, rs, l * 16, ko, 0); LD16I(x1, rs, l * 16, ko, 1024); LD16I(x2, rs, l * 16, ko, 2048); LD16I(x3, rs, l * 16, ko, 3072);
            }
        }
        SB();
        MM(4); MM(5); MM(6); MM(7); MM(8);
        v4u kf[2][4];
        if (ROLE != 2) {
#pragma unroll
            for (int u = 0; u < 4; ++u) { kf[0][u] = *(LAS v4u*)(KS + l * 16 + u * 1024); kf[1][u] = *(LAS v4u*)(KS + l * 16 + 4096 + u * 1024); }
        }
        {
            float y0 = swadd32(aq[0], ak[0]), y1 = swadd32(aq[1], ak[1]), y2 = swadd32(aq[2], ak[2]), y3 = swadd32(aq[3], ak[3]);
            v2u qk = {pkbf(y0, y1), pkbf(y2, y3)};
            *(LAS v2u*)tW = qk;
        }
        WGBAR();
        if (ROLE == 2 && w == 5) EV(h, 1);
        if (ROLE == 0) {
            if (s + 1 < NSEG) {
                v4u X[4];
#pragma unroll
                for (int u = 0; u < 4; ++u) { v2u x = trread(tR + u * 1536), y = trread(tR + u * 1536 + 128); X[u] = (v4u){x[0], x[1], y[0], y[1]}; }
                __builtin_amdgcn_sched_barrier(0);
                f4 sm = {0.f, 0.f, 0.f, 0.f}, s0 = sm, s1 = sm;
#pragma unroll
                for (int u = 0; u < 4; ++u) { sm = mf32(X[u], X[u], sm); s0 = mf32(kf[0][u], X[u], s0); s1 = mf32(kf[1][u], X[u], s1); }
                if (g < 2) sm = (f4){NINF, NINF, NINF, NINF};
                float mx = max3f(max3f(maxc(sm[0], sm[1]), maxc(sm[2], sm[3]), maxc(s0[0], s0[1])), maxc(s0[2], s0[3]), max2f(maxc(s1[0], s1[1]), maxc(s1[2], s1[3])));
                mx = xmax16(mx); mx = xmax32(mx);
                const float mb = -mx * sc2;
                const f4 t0 = s0 * sc2 + mb, t1 = s1 * sc2 + mb, tm = sm * sc2 + mb;
                f4 e0, e1, em;
#pragma unroll
                for (int q = 0; q < 4; ++q) { e0[q] = __builtin_amdgcn_exp2f(t0[q]); e1[q] = __builtin_amdgcn_exp2f(t1[q]); em[q] = __builtin_amdgcn_exp2f(tm[q]); }
                const f4 es = (e0 + e1) + em;
                float sum = (es[0] + es[1]) + (es[2] + es[3]);
                sum = xadd16(sum); sum = xadd32(sum);
                const float ri = __builtin_amdgcn_rcpf(sum);
                e0 *= ri; e1 *= ri; em *= ri;
                unsigned p0 = pkbf(e0[0], e0[1]), p1 = pkbf(e0[2], e0[3]), p2 = pkbf(e1[0], e1[1]), p3 = pkbf(e1[2], e1[3]);
                unsigned m0 = pkbf(em[0], em[1]), m1 = pkbf(em[2], em[3]);
                auto ra = __builtin_amdgcn_permlane16_swap(p0, p1, false, false);
                auto rb = __builtin_amdgcn_permlane16_swap(p2, p3, false, false);
                auto rm = __builtin_amdgcn_permlane16_swap(m0, m1, false, false);
                v4u pv = {min(ra[0], SENT - 1), min(rb[0], SENT - 1), min(ra[1], SENT - 1), min(rb[1], SENT - 1)};
                v2u mv = {min(rm[0], SENT - 1), min(rm[1], SENT - 1)};
                if (n < 8) { ST16S(pv, rs, pst0, psl); if (g >= 2) ST8S(mv, rs, pst1, psl); }
                EV(h, 2);
            }
        } else if (ROLE == 1) {
            v4u Q[4];
            asm volatile("s_waitcnt vmcnt(0)" : "+v"(x0), "+v"(x1), "+v"(x2), "+v"(x3) :: "memory");
            Q[0] = x0; Q[1] = x1; Q[2] = x2; Q[3] = x3;
            f4 a = {0.f, 0.f, 0.f, 0.f}, b = a, m = a;
#pragma unroll
            for (int u = 0; u < 4; ++u) {
                v2u x = trread(tR + u * 1536), y = trread(tR + u * 1536 + 128);
                v4u X = {x[0], x[1], y[0], y[1]};
                m = mf32(X, Q[u], m); a = mf32(kf[0][u], Q[u], a); b = mf32(kf[1][u], Q[u], b);
            }
#pragma unroll
            for (int r = 0; r < 4; ++r) { if (4 * g + r > 16 * tt + n) a[r] = NINF; if (16 + 4 * g + r > 16 * tt + n) b[r] = NINF; if (g < 2) m[r] = NINF; }
            float mx = max3f(max3f(a[0], a[1], a[2]), max3f(a[3], b[0], b[1]), max3f(b[2], b[3], m[0]));
            mx = max3f(mx, m[1], max2f(m[2], m[3]));
            mx = xmax16(mx); mx = xmax32(mx);
            const float mb = -mx * sc2;
            float ea[4], eb[4], em[4];
#pragma unroll
            for (int q = 0; q < 4; ++q) { ea[q] = __builtin_amdgcn_exp2f(fmaf(a[q], sc2, mb)); eb[q] = __builtin_amdgcn_exp2f(fmaf(b[q], sc2, mb)); em[q] = __builtin_amdgcn_exp2f(fmaf(m[q], sc2, mb)); }
            float sum = ((ea[0] + ea[1]) + (ea[2] + ea[3])) + ((eb[0] + eb[1]) + (eb[2] + eb[3])) + ((em[0] + em[1]) + (em[2] + em[3]));
            sum = xadd16(sum); sum = xadd32(sum);
            const float ri = __builtin_amdgcn_rcpf(sum);
            v4u pv = {pkbf(ea[0] * ri, ea[1] * ri), pkbf(ea[2] * ri, ea[3] * ri), pkbf(eb[0] * ri, eb[1] * ri), pkbf(eb[2] * ri, eb[3] * ri)};
            v2u mv = {pkbf(em[0] * ri, em[1] * ri), pkbf(em[2] * ri, em[3] * ri)};
            const unsigned po = WS_PF + (unsigned)s * PF_STEP;
            rdelay(6);
            ST16S(pv, rs, l * 16, po + (unsigned)(tt * 32 + h) * 1024u);
            if (g >= 2) ST8S(mv, rs, (unsigned)((h & 3) * 16 + n) * 16u + (g & 1) * 8, po + 65536u + (unsigned)(tt * 8 + (h >> 2)) * 1024u);
            VMWAIT0();
            if (l == 0) set_flag(rf, FL_P + s * 256 + h * 8 + tt * 4);
            if (h == 0 && tt == 0) TS(4, s);
        } else {
            if (w >= 6) {
                asm volatile("s_waitcnt vmcnt(0)" : "+v"(x0), "+v"(x1), "+v"(x2), "+v"(x3), "+v"(flg) :: "memory");
                const ldsp kl = L + KS_OFF + ((s + 1) & 1) * 8192 + (w & 1) * 4096 + l * 16;
                *(LAS v4u*)kl = x0; *(LAS v4u*)(kl + 1024) = x1; *(LAS v4u*)(kl + 2048) = x2; *(LAS v4u*)(kl + 3072) = x3;
                if (__builtin_expect(!__all(flg != 0u), 0)) wait_flags(P, rf, FL_QK + (s + 2) * 128 + h * 4, 1, l, 8u);
            }
            if (w == 5 && s >= 1) {
                const v4u ones = {SENT, SENT, SENT, SENT}; const v2u ones2 = {SENT, SENT};
                const unsigned prs = (unsigned)((s + 2) % 3) * PB_SLOT;
                if (n < 8) { ST16S(ones, rs, pst0, prs); if (g >= 2) ST8S(ones2, rs, pst1, prs); }
            }
        }
        if (s + 1 == NSEG) break;
        const unsigned msl = (unsigned)((s + 1) % 3) * MEM_SLOT;
        if (ROLE == 1) EV(h, 7 + w);
        const unsigned tgt = __builtin_amdgcn_readfirstlane(*(LAS unsigned*)(L + BASE_OFF)) + T_PER;
        WGBAR();
        if (s > 0) wait_until(tgt);
        if (ROLE == 2 && w == 3) EV(h, 3);
        MEM_GATHER((ROLE == 2 && w >= 6), 2u);
        if (ROLE == 2 && w == 5) EV(h, 5);
        WGBAR();
        if (ROLE == 2 && w == 5) EV(h, 6);
    }
    VMWAIT0();
}

__device__ __forceinline__ void a_body(const Params& P, int h, ldsp L) {
    const int t = threadIdx.x, w = __builtin_amdgcn_readfirstlane(t >> 6), l = t & 63;
    const rsrc_t rs = mkrs(P.ws, WS_END), rf = mkrs(P.flags, FLAG_BYTES);
    v8u WQ[9], WK[9];
    stage_w128(P.Wq, HE, h * EH, L, t);
#pragma unroll
    for (int kb = 0; kb < 9; ++kb) WQ[kb] = bfrag_sp(L, 256, kb * 64, w, l, 7);
    stage_w128(P.Wk, HE, h * EH, L, t);
#pragma unroll
    for (int kb = 0; kb < 9; ++kb) WK[kb] = bfrag_sp(L, 256, kb * 64, w, l, 7);
    __syncthreads();
    load_mem0(P.mem0, L, t);
    const unsigned par = rec_wait_init(P, L, t);
    if (w == 1) wait_flags(P, rf, FL_QK + h * 4, 1, l, 16u);
    if (w == 2) wait_flags(P, rf, FL_QK + 128 + h * 4, 1, l, 16u);
    __syncthreads();
    *(LAS v4u*)(L + KS_OFF + t * 16) = ldS(rs, WS_KF + (unsigned)h * 8192u + t * 16, 0);
    __syncthreads();
    if (w == 0) a_loop<0>(P, h, L, WQ, WK, par); else if (w < 3) a_loop<1>(P, h, L, WQ, WK, par); else a_loop<2>(P, h, L, WQ, WK, par);
}

template <bool FIVE>
__device__ __forceinline__ void b_loop(const Params& P, int h, ldsp L, const v8u (&WV)[9], const v8u (&WO)[5][2], unsigned par) {
    const int t = threadIdx.x, w = __builtin_amdgcn_readfirstlane(t >> 6), l = t & 63, n = l & 15, g = l >> 4;
    const rsrc_t rs = mkrs(P.ws, WS_END), rf = mkrs(P.flags, FLAG_BYTES);
    const unsigned pa = par << 13;
    const int sidx = (n & 8) ? 0xEEEE : 0x4444;
    const int q4 = n >> 2, p4 = n & 3;
    const int aoff = (16 * g + 8 * (p4 >> 1) + q4) * 16 + g * 64 + (p4 & 1) * 8;
    const ldsp memA = L + MEMT_OFF + aoff, vmA = L + T_OFF + aoff;
    const ldsp vmW = L + T_OFF + (16 * w + n) * 16 + w * 64 + (g & 1) * 8 + (g >> 1) * 4;
    const unsigned mg0 = MEM_BASE + t * 16 + pa, mg1 = MEM_BASE + 16384 + (w == 7 ? l * 16 : 1024) + pa;
    const ldsp mw0 = L + MEMT_OFF + t * 16 + (t >> 4) * 64, mw1 = (w == 7) ? L + MEMT_OFF + (512 + l) * 16 + ((512 + l) >> 4) * 64 : L + BASE_OFF;
    const int ptile = 4 * w + ((g & 1) << 1) + (g >> 1);
    const unsigned us0 = (unsigned)ptile * 16384u + h * 256 + n * 16 + pa;
    const unsigned us1 = (unsigned)(32 + (w & 3)) * 16384u + h * 256 + n * 16 + (g & 1) * 8 + (g >> 1) * 4 + pa;
    const unsigned up0 = WS_UM + (unsigned)ptile * 8192u + h * 256 + n * 16;
    const unsigned up1 = WS_UM + (unsigned)(32 + (w & 3)) * 8192u + h * 256 + n * 16 + (g & 1) * 8 + (g >> 1) * 4;
    v4u ov = {0u, 0u, 0u, 0u}; unsigned pe = 0u;
    for (int s = 0; s < NSEG; ++s) {
        const unsigned umo = (unsigned)(s % 3) * UM_SLOT;
        if (w == 1) EV(h, 10);
        v4u A[9];
        f4 av = {0.f, 0.f, 0.f, 0.f};
        RD(0); RD(1); RD(2); RD(3); SB();
        av = smf(A[0], WV[0], av, sidx); SB(); RD(4); RD(5); RD(6); RD(7); RD(8); SB();
#pragma unroll
        for (int kb = 1; kb < 9; ++kb) av = smf(A[kb], WV[kb], av, sidx);
        { float s01 = swadd32(av[0], av[1]), s23 = swadd32(av[2], av[3]);
          auto rr = __builtin_amdgcn_permlane32_swap(f2u(s01), f2u(s23), false, false);
          *(LAS unsigned*)vmW = pkbf(u2f(rr[0]), u2f(rr[1])); }
        WGBAR();
        {
            v4u oa[2];
#pragma unroll
            for (int kb = 0; kb < 2; ++kb) { v2u x = trread(vmA + kb * 1280), y = trread(vmA + kb * 1280 + 64); oa[kb] = (v4u){x[0], x[1], y[0], y[1]}; }
            f4 z = {0.f, 0.f, 0.f, 0.f};
            f4 d[5];
#pragma unroll
            for (int i = 0; i < (FIVE ? 5 : 4); ++i) { d[i] = smf(oa[0], WO[i][0], z, sidx); }
#pragma unroll
            for (int i = 0; i < (FIVE ? 5 : 4); ++i) { d[i] = smf(oa[1], WO[i][1], d[i], sidx); }
            unsigned xp0 = pkbf(swadd32(d[0][0], d[1][0]), swadd32(d[0][1], d[1][1])), xp1 = pkbf(swadd32(d[0][2], d[1][2]), swadd32(d[0][3], d[1][3]));
            unsigned yp0 = pkbf(swadd32(d[2][0], d[3][0]), swadd32(d[2][1], d[3][1])), yp1 = pkbf(swadd32(d[2][2], d[3][2]), swadd32(d[2][3], d[3][3]));
            auto r0 = __builtin_amdgcn_permlane16_swap(xp0, yp0, false, false);
            auto r1 = __builtin_amdgcn_permlane16_swap(xp1, yp1, false, false);
            ov = (v4u){min(r0[0], SENT - 1), min(r1[0], SENT - 1), min(r0[1], SENT - 1), min(r1[1], SENT - 1)};
            if (s + 1 < NSEG) ST16S(ov, rs, us0, umo);
            if (FIVE) {
                float s01 = swadd32(d[4][0], d[4][1]), s23 = swadd32(d[4][2], d[4][3]);
                auto rr = __builtin_amdgcn_permlane32_swap(f2u(s01), f2u(s23), false, false);
                pe = min(pkbf(u2f(rr[0]), u2f(rr[1])), SENT - 1);
                if (s + 1 < NSEG) ST4S(pe, rs, us1, umo);
            }
        }
        if (w == 5) EV(h, 11);
        const unsigned base = __builtin_amdgcn_readfirstlane(*(LAS unsigned*)(L + BASE_OFF));
        if (s > 0 && s + 1 < NSEG) wait_until(base + C_P + C_B);
        ST16S(ov, rs, up0, (unsigned)s * UMP_STEP); if (FIVE) ST4S(pe, rs, up1, (unsigned)s * UMP_STEP);
        if (s + 1 == NSEG) { VMWAIT0(); WGBAR(); if (t == 64) set_flag(rf, FL_UM + s * 128 + h * 4); break; }
        {
            const unsigned umr = (unsigned)((s + 2) % 3) * UM_SLOT;
            const v4u ones = {SENT, SENT, SENT, SENT};
            ST16S(ones, rs, us0, umr); if (FIVE) ST4S(ones[0], rs, us1, umr);
        }
        if (w == 2) EV(h, 12);
        WGBAR();
        if (s > 0) wait_until(base + T_PER);
        if (w == 0) EV(h, 13);
        const unsigned msl = (unsigned)((s + 1) % 3) * MEM_SLOT;
        MEM_GATHER((!FIVE && w >= 6), 32u);
        WGBAR();
        if (t == 64) set_flag(rf, FL_UM + s * 128 + h * 4);
        if (w == 2) EV(h, 14);
    }
    VMWAIT0();
}

__device__ __forceinline__ void b_body(const Params& P, int h, ldsp L) {
    const int t = threadIdx.x, w = __builtin_amdgcn_readfirstlane(t >> 6), l = t & 63;
    v8u WV[9], WO[5][2];
    stage_w128(P.Wv, HE, h * EH, L, t);
#pragma unroll
    for (int kb = 0; kb < 9; ++kb) WV[kb] = bfrag_sp(L, 256, kb * 64, w, l, 7);
#pragma unroll
    for (int kb = 0; kb < 2; ++kb) {
        stage_wo64(P.Wo, h * EH + kb * 64, L, t);
#pragma unroll
        for (int i = 0; i < 4; ++i) WO[i][kb] = bfrag_sp(L, 1184, 0, 4 * w + i, l, 0);
        WO[4][kb] = bfrag_sp(L, 1184, 0, 32 + (w & 3), l, 0);
    }
    __syncthreads();
    load_mem0(P.mem0, L, t);
    const unsigned par = rec_wait_init(P, L, t);
    if (w < 4) b_loop<true>(P, h, L, WV, WO, par); else b_loop<false>(P, h, L, WV, WO, par);
}

template <bool MEMF>
__device__ __forceinline__ void r_loop(const Params& P, int r, ldsp L, unsigned par) {
    const int t = threadIdx.x, w = __builtin_amdgcn_readfirstlane(t >> 6), l = t & 63, n = l & 15, g = l >> 4;
    const rsrc_t rs = mkrs(P.ws, WS_END), rf = mkrs(P.flags, FLAG_BYTES);
    const unsigned pa = par << 13;
    const int cg = r >> 1, rh = r & 1;
    const int sidx = (n & 8) ? 0xEEEE : 0x4444;
    auto pmap = [&](unsigned off) { return PB_BASE + ((off >> 13) << 14) + (off & 8191u) + pa; };
    const unsigned pl0 = pmap((unsigned)rh * 10240u + (unsigned)w * 1024u + l * 16), pl1 = pmap((unsigned)rh * 10240u + (unsigned)(8 + (w & 1)) * 1024u + l * 16);
    const unsigned usb = WS_US + (unsigned)(2 * cg) * 32768u + (unsigned)w * 4096u;
    const unsigned umb = (unsigned)(2 * cg) * 16384u + (unsigned)(w & 1) * 4096u + pa;
    const int md = cg * 32 + 16 * (g >> 1) + n;
    const unsigned ms = MEM_BASE + (md < 512 ? md * 16 : 16384 + (md - 512) * 16) + rh * 8 + (g & 1) * 4 + pa;
    const unsigned l16 = l * 16;
    const unsigned anc = MEM_BASE + 16384 + 1024 + pa;
    unsigned base = 0u;
    for (int s = 0; s + 1 < NSEG; ++s) {
        const unsigned psl = (unsigned)(s % 3) * PB_SLOT;
        if (w == 2) EV(r, 20);
        v4u b[2][4]; unsigned flg = 1u;
        const unsigned uso = usb + (unsigned)s * USEG_STEP;
        LD16I(b[0][0], rs, l16, uso, 0); LD16I(b[0][1], rs, l16, uso, 1024); LD16I(b[0][2], rs, l16, uso, 2048); LD16I(b[0][3], rs, l16, uso, 3072);
        LD16I(b[1][0], rs, l16, uso + 32768u, 0); LD16I(b[1][1], rs, l16, uso + 32768u, 1024); LD16I(b[1][2], rs, l16, uso + 32768u, 2048); LD16I(b[1][3], rs, l16, uso + 32768u, 3072);
        LD4(flg, rf, FL_VU + (unsigned)(4 * w + (l & 3)) * 4u, (unsigned)(s + 1) * 128u);
        asm volatile("s_waitcnt vmcnt(0)" : "+v"(b[0][0]), "+v"(b[0][1]), "+v"(b[0][2]), "+v"(b[0][3]), "+v"(b[1][0]), "+v"(b[1][1]), "+v"(b[1][2]), "+v"(b[1][3]), "+v"(flg) :: "memory");
        if (__builtin_expect(!__all(flg != 0u), 0)) wait_flags(P, rf, FL_VU + (s + 1) * 128 + w * 16, 4, l, 4u);
        if (w == 7) EV(r, 21);
        if (!MEMF && w == 0 && s > 0) {
            if (r != 0) {
                for (int sp = 0;; ++sp) {
                    unsigned bv; LD4(bv, rs, anc, (unsigned)(s % 3) * MEM_SLOT);
                    asm volatile("s_waitcnt vmcnt(0)" : "+v"(bv) :: "memory");
                    base = __builtin_amdgcn_readfirstlane(bv);
                    if (base != SENT || sp > 64) break;
                }
            }
            EV(r, 22);
            if (l == 0) *(LAS unsigned*)(L + BASE_OFF) = base;
        }
        WGBAR();
        const unsigned bs = __builtin_amdgcn_readfirstlane(*(LAS unsigned*)(L + BASE_OFF));
        if (MEMF && s > 0) wait_until(bs + C_U);
        if (w == 2) EV(r, 23);
        v4u u[2][4];
        const unsigned umo = umb + (unsigned)(s % 3) * UM_SLOT;
        if (MEMF) {
            LD16I(u[0][0], rs, l16, umo, 0); LD16I(u[0][1], rs, l16, umo, 1024); LD16I(u[0][2], rs, l16, umo, 2048); LD16I(u[0][3], rs, l16, umo, 3072);
            LD16I(u[1][0], rs, l16, umo + 16384u, 0); LD16I(u[1][1], rs, l16, umo + 16384u, 1024); LD16I(u[1][2], rs, l16, umo + 16384u, 2048); LD16I(u[1][3], rs, l16, umo + 16384u, 3072);
        }
        if (s > 0) wait_until(bs + C_P);
        v4u a0, a1;
        LD16(a0, rs, pl0, psl);
        if (MEMF) LD16(a1, rs, pl1, psl);
        f4 cx[2], cy[2];
#define R_WAIT() do { if (MEMF) asm volatile("s_waitcnt vmcnt(0)" : "+v"(a0), "+v"(a1), "+v"(u[0][0]), "+v"(u[0][1]), "+v"(u[0][2]), "+v"(u[0][3]), "+v"(u[1][0]), "+v"(u[1][1]), "+v"(u[1][2]), "+v"(u[1][3]) :: "memory"); \
                      else asm volatile("s_waitcnt vmcnt(0)" : "+v"(a0) :: "memory"); } while (0)
#define R_BAD() (!__all((MEMF ? max(max(umax4(a0), umax4(a1)), max(max(max(umax4(u[0][0]), umax4(u[0][1])), max(umax4(u[0][2]), umax4(u[0][3]))), max(max(umax4(u[1][0]), umax4(u[1][1])), max(umax4(u[1][2]), umax4(u[1][3]))))) : umax4(a0)) != SENT))
#define R_MMA() do { const f4 z = {0.f, 0.f, 0.f, 0.f}; \
            _Pragma("unroll") for (int c = 0; c < 2; ++c) { cx[c] = smf(a0, cat8(b[c][0], b[c][1]), z, sidx); cy[c] = smf(a0, cat8(b[c][2], b[c][3]), z, sidx); } \
            if (MEMF) { _Pragma("unroll") for (int c = 0; c < 2; ++c) { cx[c] = smf(a1, cat8(u[c][0], u[c][1]), cx[c], sidx); cy[c] = smf(a1, cat8(u[c][2], u[c][3]), cy[c], sidx); } } } while (0)
        unsigned mu = 0u;
        if (MEMF) {
            asm volatile("s_waitcnt vmcnt(2)" : "+v"(u[0][0]), "+v"(u[0][1]), "+v"(u[0][2]), "+v"(u[0][3]), "+v"(u[1][0]), "+v"(u[1][1]), "+v"(u[1][2]), "+v"(u[1][3]) :: "memory");
            mu = max(max(max(umax4(u[0][0]), umax4(u[0][1])), max(umax4(u[0][2]), umax4(u[0][3]))), max(max(umax4(u[1][0]), umax4(u[1][1])), max(umax4(u[1][2]), umax4(u[1][3]))));
            asm volatile("s_waitcnt vmcnt(0)" : "+v"(a0), "+v"(a1), "+v"(mu) :: "memory");
        } else asm volatile("s_waitcnt vmcnt(0)" : "+v"(a0) :: "memory");
        R_MMA();
        if (w == 3) EV(r, 25); if (w == 7) EV(r, 26);
        if (__builtin_expect(!__all((MEMF ? max(max(umax4(a0), umax4(a1)), mu) : umax4(a0)) != SENT), 0)) {
            EV(r, 32 + w);
            for (int sp = 0;; ++sp) {
                if (MEMF) {
                    LD16I(u[0][0], rs, l16, umo, 0); LD16I(u[0][1], rs, l16, umo, 1024); LD16I(u[0][2], rs, l16, umo, 2048); LD16I(u[0][3], rs, l16, umo, 3072);
                    LD16I(u[1][0], rs, l16, umo + 16384u, 0); LD16I(u[1][1], rs, l16, umo + 16384u, 1024); LD16I(u[1][2], rs, l16, umo + 16384u, 2048); LD16I(u[1][3], rs, l16, umo + 16384u, 3072);
                    LD16(a1, rs, pl1, psl);
                }
                LD16(a0, rs, pl0, psl);
                R_WAIT();
                if (!R_BAD()) break;
                if (spin_fail(P, sp, l, 1u)) break;
            }
            R_MMA();
        }
        {
            float x[4];
#pragma unroll
            for (int q = 0; q < 4; ++q) x[q] = swadd32((g & 1) ? cy[0][q] : cx[0][q], (g & 1) ? cy[1][q] : cx[1][q]);
            auto r01 = __builtin_amdgcn_permlane16_swap(f2u(x[0]), f2u(x[1]), false, false);
            auto r23 = __builtin_amdgcn_permlane16_swap(f2u(x[2]), f2u(x[3]), false, false);
            f2 hs = {u2f(r01[0]) + u2f(r01[1]), u2f(r23[0]) + u2f(r23[1])};
            *(LAS f2*)(L + RED_OFF + w * 512 + l * 8) = hs;
        }
        WGBAR();
        if (w == 2) EV(r, 27);
        if (!MEMF && w == 0) {
            f2 v[8];
#pragma unroll
            for (int i = 0; i < 8; ++i) v[i] = *(LAS f2*)(L + RED_OFF + i * 512 + l * 8);
            f2 sum = ((v[0] + v[1]) + (v[2] + v[3])) + ((v[4] + v[5]) + (v[6] + v[7]));
            auto rr = __builtin_amdgcn_permlane16_swap(f2u(sum[0]), f2u(sum[1]), false, false);
            unsigned pk = min(pkbf(u2f(rr[0]), u2f(rr[1])), SENT - 1);
            ST4S(pk, rs, ms, (unsigned)((s + 1) % 3) * MEM_SLOT);
            if (r == 0) {
                const unsigned now = (unsigned)__builtin_amdgcn_s_memrealtime();
                unsigned nb = base + T_PER;
                if (s == 0 || (int)(now - nb) > -4) nb = now + C_SLIP;
                if (nb == SENT) nb = 0u;
                base = nb;
                const v4u bv = {nb, nb, nb, nb}, ones = {SENT, SENT, SENT, SENT};
                if (l == 0) { ST16S(bv, rs, anc, (unsigned)((s + 1) % 3) * MEM_SLOT); ST16S(ones, rs, anc, (unsigned)(s % 3) * MEM_SLOT); }
            }
            ST4S(SENT, rs, ms, (unsigned)(s % 3) * MEM_SLOT);
            EV(r, 28);
        }
        WGBAR();
    }
    VMWAIT0();
}

__device__ __forceinline__ void r_body(const Params& P, int r, ldsp L) {
    const int t = threadIdx.x, w = __builtin_amdgcn_readfirstlane(t >> 6), l = t & 63;
    const rsrc_t rs = mkrs(P.ws, WS_END), rf = mkrs(P.flags, FLAG_BYTES);
    if (r == 0 && t == 0) {
        unsigned best[2];
#pragma unroll
        for (int p = 0; p < 2; ++p) {
            GAS unsigned* a = (GAS unsigned*)(P.ws + CAL_BASE + p * 8192);
            unsigned b = 1u << 30;
            for (int i = 0; i < 6; ++i) {
                unsigned long long t0 = __builtin_amdgcn_s_memtime();
                __hip_atomic_store(a, (unsigned)i, __ATOMIC_RELAXED, __HIP_MEMORY_SCOPE_AGENT);
                VMWAIT0();
                unsigned d = (unsigned)(__builtin_amdgcn_s_memtime() - t0); b = d < b ? d : b;
            }
            best[p] = b;
        }
        __hip_atomic_store((GAS unsigned*)P.flags + FL_PAR / 4, best[0] < best[1] ? 1u : 2u, __ATOMIC_RELAXED, __HIP_MEMORY_SCOPE_AGENT);
    }
    __syncthreads();
    {
        const v4u ones = {SENT, SENT, SENT, SENT};
#pragma unroll
        for (int i = 0; i < 7; ++i) { unsigned o = (unsigned)(i * 512 + t) * 16u; unsigned a = (unsigned)r * 56448u + o; if (o < 56448u && a < CAL_BASE) stS(ones, rs, a, 0); }
    }
    const unsigned par = rec_handshake(P, L, t);
    wait_flags(P, rf, FL_VU + w * 16, 4, l, 16u);
    if (w < 6) r_loop<false>(P, r, L, par); else r_loop<true>(P, r, L, par);
}

__device__ __forceinline__ v4u dfrag(ldsp stg, int rowbytes, int k0, int cb, int pstep, int l, int swzmask) {
    const int g = l >> 4, q = (l & 15) >> 2, p = l & 3;
    const int d0 = k0 + 8 * g + q, d1 = d0 + 4;
    const int cbyte = cb + p * pstep;
    v2u x = trread(stg + d0 * rowbytes + (cbyte ^ ((d0 & swzmask) << 5)));
    v2u y = trread(stg + d1 * rowbytes + (cbyte ^ ((d1 & swzmask) << 5)));
    v4u r = {x[0], x[1], y[0], y[1]};
    asm volatile("" : "+v"(r));
    return r;
}
constexpr int AT_OFF = 0;
constexpr int XC_OFF = 73728;
constexpr int BR_OFF = 49152;

#define TILE_LOAD(T, base) do { _Pragma("unroll") for (int i = 0; i < 4; ++i) T[i] = ldS(rs, l * 16, (base) + (unsigned)(w + 8 * i) * 1024u); \
        if (w < 4) T[4] = ldS(rs, l * 16, (base) + (unsigned)(32 + w) * 1024u); } while (0)
#define TILE_STORE(T) do { _Pragma("unroll") for (int i = 0; i < 4; ++i) *(LAS v4u*)(AT + (w + 8 * i) * 1024 + l * 16) = T[i]; \
        if (w < 4) *(LAS v4u*)(AT + (32 + w) * 1024 + l * 16) = T[4]; } while (0)

#define KLOOP(BODY) do { v4u Bf[3][2]; \
        _Pragma("unroll") for (int i_ = 0; i_ < 2; ++i_) { Bf[i_][0] = *(LAS v4u*)(AT + i_ * 1024 + l * 16); Bf[i_][1] = *(LAS v4u*)(AT + (18 + i_) * 1024 + l * 16); } \
        _Pragma("unroll") for (int ks = 0; ks < 18; ++ks) { \
            if (ks + 2 < 18) { Bf[(ks + 2) % 3][0] = *(LAS v4u*)(AT + (ks + 2) * 1024 + l * 16); Bf[(ks + 2) % 3][1] = *(LAS v4u*)(AT + (20 + ks) * 1024 + l * 16); } \
            __builtin_amdgcn_sched_barrier(0); \
            const v4u b0 = Bf[ks % 3][0], b1 = Bf[ks % 3][1]; \
            BODY \
            __builtin_amdgcn_sched_barrier(0); \
        } } while (0)
__device__ __forceinline__ void wait_h(const Params& P, rsrc_t rf, unsigned long long& mask, int s, int l) {
    for (int sp = 0; !((mask >> s) & 1ull); ++sp) {
        unsigned fl; LD4(fl, rf, FL_H + l * 4, 0u);
        asm volatile("s_waitcnt vmcnt(0)" : "+v"(fl) :: "memory");
        mask = __ballot(fl != 0u);
        if ((mask >> s) & 1ull) break;
        if (spin_fail(P, sp, l, 0x200u)) break;
        __builtin_amdgcn_s_sleep(1);
    }
}
__device__ __forceinline__ void qk_body(const Params& P, int h, ldsp L) {
    const int t = threadIdx.x, w = __builtin_amdgcn_readfirstlane(t >> 6), l = t & 63;
    const rsrc_t rs = mkrs(P.ws, WS_END), rf = mkrs(P.flags, FLAG_BYTES);
    const int u = w & 3;
    v4u W[2][18];
#pragma unroll
    for (int m = 0; m < 2; ++m) {
        stage_w128(m == 0 ? P.Wk : P.Wq, HE, h * EH, L, t);
        if ((w >> 2) == m) {
#pragma unroll
            for (int ks = 0; ks < 18; ++ks) { W[0][ks] = dfrag(L, 256, ks * 32, u * 64, 16, l, 7); W[1][ks] = dfrag(L, 256, ks * 32, u * 64 + 8, 16, l, 7); }
        }
    }
    const unsigned ob = ((w >> 2) == 0 ? WS_KF : WS_QF) + (unsigned)h * 8192u + (unsigned)u * 1024u + l * 16;
    v4u T[5];
    unsigned long long hmask = 0ull;
    wait_h(P, rf, hmask, 0, l);
    __syncthreads();
    TILE_LOAD(T, WS_HF);
    if (w < 4) __builtin_amdgcn_s_setprio(1);
    for (int s = 0; s < NSEG; ++s) {
        const ldsp AT = L + AT_OFF + (s & 1) * 36864;
        TILE_STORE(T);
        if (s + 1 < NSEG) { wait_h(P, rf, hmask, s + 1, l); TILE_LOAD(T, WS_HF + (unsigned)(s + 1) * TF_STEP); }
        WGBAR();
        if (t == 0 && s >= 5) set_flag(rf, FL_QK + (s - 2) * 128 + h * 4);
        if (t == 0 && h == 0) TS(1, s);
        f4 c[2][2];
#pragma unroll
        for (int a = 0; a < 2; ++a) { c[a][0] = (f4){0.f, 0.f, 0.f, 0.f}; c[a][1] = c[a][0]; }
        KLOOP(c[0][0] = mf32(W[0][ks], b0, c[0][0]); c[1][0] = mf32(W[1][ks], b0, c[1][0]);
              c[0][1] = mf32(W[0][ks], b1, c[0][1]); c[1][1] = mf32(W[1][ks], b1, c[1][1]););
        asm volatile("s_waitcnt vmcnt(0)" : "+v"(c[0][0]), "+v"(c[0][1]), "+v"(c[1][0]), "+v"(c[1][1]) :: "memory");
#pragma unroll
        for (int tt = 0; tt < 2; ++tt) {
            v4u o = {pkbf(c[0][tt][0], c[0][tt][1]), pkbf(c[0][tt][2], c[0][tt][3]), pkbf(c[1][tt][0], c[1][tt][1]), pkbf(c[1][tt][2], c[1][tt][3])};
            stS(o, rs, ob + tt * 4096u, (unsigned)s * (NH * 8192u));
        }
        if (s < 3) { VMWAIT0(); WGBAR(); if (t == 0) set_flag(rf, FL_QK + s * 128 + h * 4); }
    }
    VMWAIT0(); WGBAR();
    if (t == 0) { set_flag(rf, FL_QK + (NSEG - 2) * 128 + h * 4); set_flag(rf, FL_QK + (NSEG - 1) * 128 + h * 4); }
}

template <bool FIVE>
__device__ __forceinline__ void vu_loop(const Params& P, int h, ldsp L, const v4u (&WV)[18], const v4u (&WO)[5][4]) {
    const int t = threadIdx.x, w = __builtin_amdgcn_readfirstlane(t >> 6), l = t & 63;
    const rsrc_t rs = mkrs(P.ws, WS_END), rf = mkrs(P.flags, FLAG_BYTES);
    const unsigned ob = WS_US + (unsigned)(4 * w) * 32768u + (unsigned)h * 1024u + l * 16;
    const unsigned ob5 = WS_US + (unsigned)(32 + (w & 3)) * 32768u + (unsigned)h * 1024u + l * 16;
    v4u T[5];
    unsigned long long hmask = 0ull;
    wait_h(P, rf, hmask, 0, l);
    TILE_LOAD(T, WS_HF);
    if (FIVE) __builtin_amdgcn_s_setprio(1);
    for (int s = 0; s < NSEG; ++s) {
        const ldsp AT = L + AT_OFF;
        TILE_STORE(T);
        if (s + 1 < NSEG) { wait_h(P, rf, hmask, s + 1, l); TILE_LOAD(T, WS_HF + (unsigned)(s + 1) * TF_STEP); }
        WGBAR();
        if (t == 0 && h == 0) TS(2, s);
        f4 c0 = {0.f, 0.f, 0.f, 0.f}, c1 = c0;
        KLOOP(c0 = mf32(WV[ks], b0, c0); c1 = mf32(WV[ks], b1, c1););
        { const ldsp xp = L + XC_OFF + (w >> 1) * 1024 + l * 16 + (w & 1) * 8;
          *(LAS v2u*)xp = (v2u){pkbf(c0[0], c0[1]), pkbf(c0[2], c0[3])}; *(LAS v2u*)(xp + 4096) = (v2u){pkbf(c1[0], c1[1]), pkbf(c1[2], c1[3])}; }
        VMWAIT0();
        WGBAR();
        if (t == 0 && s > 2) set_flag(rf, FL_VU + (s - 1) * 128 + h * 4);
        v4u A[2][4];
#pragma unroll
        for (int mt = 0; mt < 2; ++mt)
#pragma unroll
            for (int ks = 0; ks < 4; ++ks) A[mt][ks] = *(LAS v4u*)(L + XC_OFF + mt * 4096 + ks * 1024 + l * 16);
#pragma unroll
        for (int i = 0; i < (FIVE ? 5 : 4); ++i) {
            f4 d0 = {0.f, 0.f, 0.f, 0.f}, d1 = d0;
#pragma unroll
            for (int ks = 0; ks < 4; ++ks) { d0 = mf32(A[0][ks], WO[i][ks], d0); d1 = mf32(A[1][ks], WO[i][ks], d1); }
            v4u o = {pkbf(d0[0], d0[1]), pkbf(d0[2], d0[3]), pkbf(d1[0], d1[1]), pkbf(d1[2], d1[3])};
            if (i < 4) stS(o, rs, ob, (unsigned)s * USEG_STEP + (unsigned)i * 32768u); else stS(o, rs, ob5, (unsigned)s * USEG_STEP);
        }
        if (s < 2) { VMWAIT0(); WGBAR(); if (t == 0) set_flag(rf, FL_VU + s * 128 + h * 4); }
    }
    VMWAIT0(); WGBAR();
    if (t == 0) set_flag(rf, FL_VU + (NSEG - 1) * 128 + h * 4);
}
__device__ __forceinline__ void vu_body(const Params& P, int h, ldsp L) {
    const int t = threadIdx.x, w = __builtin_amdgcn_readfirstlane(t >> 6), l = t & 63;
    v4u WV[18], WO[5][4];
    stage_w128(P.Wv, HE, h * EH, L, t);
#pragma unroll
    for (int ks = 0; ks < 18; ++ks) WV[ks] = dfrag(L, 256, ks * 32, (w >> 1) * 64 + (w & 1) * 8, 16, l, 7);
#pragma unroll
    for (int kb = 0; kb < 2; ++kb) {
        stage_wo64(P.Wo, h * EH + kb * 64, L, t);
#pragma unroll
        for (int k2 = 0; k2 < 2; ++k2) {
#pragma unroll
            for (int i = 0; i < 4; ++i) WO[i][kb * 2 + k2] = dfrag(L, 1184, k2 * 32, (4 * w + i) * 32, 8, l, 0);
            WO[4][kb * 2 + k2] = dfrag(L, 1184, k2 * 32, (32 + (w & 3)) * 32, 8, l, 0);
        }
    }
    __syncthreads();
    if (w < 4) vu_loop<true>(P, h, L, WV, WO); else vu_loop<false>(P, h, L, WV, WO);
}

__device__ __forceinline__ void mod_slice(const Params& P, int m, ldsp L) {
    const int t = threadIdx.x, kq = t >> 4, cc = t & 15;
    const rsrc_t rs = mkrs(P.ws, WS_END), rf = mkrs(P.flags, FLAG_BYTES);
    v4u wv[18];
#pragma unroll
    for (int i = 0; i < 18; ++i) wv[i] = *(const v4u*)(P.wmod + (size_t)(kq * 18 + i) * (6 * DM) + m * 128 + cc * 8);
    float acc[8] = {0.f, 0.f, 0.f, 0.f, 0.f, 0.f, 0.f, 0.f};
#pragma unroll
    for (int i = 0; i < 18; ++i) {
        float cv = blo((unsigned)P.c[kq * 18 + i]);
        float sv = rbf(cv / (1.f + __expf(-cv)));
#pragma unroll
        for (int j = 0; j < 4; ++j) { acc[2 * j] = fmaf(sv, blo(wv[i][j]), acc[2 * j]); acc[2 * j + 1] = fmaf(sv, bhi(wv[i][j]), acc[2 * j + 1]); }
    }
    * (LAS f4*)(L + kq * 512 + cc * 32) = (f4){acc[0], acc[1], acc[2], acc[3]};
    * (LAS f4*)(L + kq * 512 + cc * 32 + 16) = (f4){acc[4], acc[5], acc[6], acc[7]};
    __syncthreads();
    if (t < 128) {
        float sacc = 0.f;
#pragma unroll
        for (int k = 0; k < 32; ++k) sacc += *(LAS float*)(L + k * 512 + t * 4);
        __builtin_amdgcn_raw_buffer_store_b32(f2u(rbf(sacc)), rs, WS_MOD + (unsigned)(m * 128 + t) * 4u, 0, 16);
    }
    VMWAIT0();
    __syncthreads();
    if (t == 0) set_flag(rf, FL_MOD + m * 4);
    if (t == 0) TS(9, m);
}

__device__ __forceinline__ void ln1_body(const Params& P, int i0, int stride, ldsp L) {
    const int t = threadIdx.x, w = __builtin_amdgcn_readfirstlane(t >> 6), l = t & 63, j = l & 15;
    const rsrc_t rs = mkrs(P.ws, WS_END), rf = mkrs(P.flags, FLAG_BYTES);
    const int row = 4 * w + (l >> 4), tt = row >> 4, n = row & 15;
    bool have_mod = false;
    constexpr int MOD_OFF = 40960;
    for (int s = i0; s < NSEG; s += stride) {
        v4u xr[5];
        const u16* xp = P.x + (size_t)(s * 32 + row) * DM;
#pragma unroll
        for (int i = 0; i < 4; ++i) xr[i] = *(const v4u*)(xp + (j + 16 * i) * 8);
        xr[4] = (v4u){0u, 0u, 0u, 0u};
        if (j < 8) xr[4] = *(const v4u*)(xp + (64 + j) * 8);
        float sum = 0.f;
#pragma unroll
        for (int i = 0; i < 5; ++i)
#pragma unroll
            for (int k = 0; k < 4; ++k) sum += blo(xr[i][k]) + bhi(xr[i][k]);
        const float mu = rowsum16(sum) * (1.f / DM);
        float q = 0.f;
#pragma unroll
        for (int i = 0; i < 5; ++i)
#pragma unroll
            for (int k = 0; k < 4; ++k) { float a = blo(xr[i][k]) - mu, b = bhi(xr[i][k]) - mu; if (i < 4 || j < 8) q += a * a + b * b; }
        const float rstd = 1.f / sqrtf(rowsum16(q) * (1.f / DM) + 1e-5f);
        if (!have_mod) {
            if (w == 0) wait_flags(P, rf, FL_MOD, 9, l, 0x400u);
            __syncthreads();
            for (int i = t; i < 2 * DM; i += 512) {
                float v = u2f(__builtin_amdgcn_raw_buffer_load_b32(rs, WS_MOD + (unsigned)i * 4u, 0, 16));
                if (i >= DM) *(LAS float*)(L + MOD_OFF + (i - DM) * 4) = rbf(1.f + v); else *(LAS float*)(L + MOD_OFF + (DM + i) * 4) = v;
            }
            have_mod = true;
        }
        __syncthreads();
#pragma unroll
        for (int i = 0; i < 5; ++i) {
            const int c = (i < 4) ? j + 16 * i : 64 + j;
            if (i < 4 || j < 8) {
                f4 g0 = *(LAS f4*)(L + MOD_OFF + c * 32), g1 = *(LAS f4*)(L + MOD_OFF + c * 32 + 16);
                f4 s0 = *(LAS f4*)(L + MOD_OFF + DM * 4 + c * 32), s1 = *(LAS f4*)(L + MOD_OFF + DM * 4 + c * 32 + 16);
                v4u o;
                o[0] = pkbf(fmaf((blo(xr[i][0]) - mu) * rstd, g0[0], s0[0]), fmaf((bhi(xr[i][0]) - mu) * rstd, g0[1], s0[1]));
                o[1] = pkbf(fmaf((blo(xr[i][1]) - mu) * rstd, g0[2], s0[2]), fmaf((bhi(xr[i][1]) - mu) * rstd, g0[3], s0[3]));
                o[2] = pkbf(fmaf((blo(xr[i][2]) - mu) * rstd, g1[0], s1[0]), fmaf((bhi(xr[i][2]) - mu) * rstd, g1[1], s1[1]));
                o[3] = pkbf(fmaf((blo(xr[i][3]) - mu) * rstd, g1[2], s1[2]), fmaf((bhi(xr[i][3]) - mu) * rstd, g1[3], s1[3]));
                *(LAS v4u*)(L + AT_OFF + (tt * 18 + (c >> 2)) * 1024 + (c & 3) * 256 + ((n ^ (c & 15)) * 16)) = o;
            }
        }
        __syncthreads();
#pragma unroll
        for (int i = 0; i < 5; ++i) {
            const int f = w + 8 * i;
            if (f < 36) {
                const int ks = f % 18, g = l >> 4;
                v4u o = *(LAS v4u*)(L + AT_OFF + f * 1024 + g * 256 + ((j ^ ((4 * ks + g) & 15)) * 16));
                stS(o, rs, WS_HF + (unsigned)f * 1024u + l * 16, (unsigned)s * TF_STEP);
            }
        }
        VMWAIT0();
        __syncthreads();
        if (t == 0) set_flag(rf, FL_H + s * 4);
        if (t == 0) TS(3, s);
    }
}

__device__ __forceinline__ void pu_body(const Params& P, int q, ldsp L) {
    const int t = threadIdx.x, w = __builtin_amdgcn_readfirstlane(t >> 6), l = t & 63, n = l & 15, g = l >> 4;
    const rsrc_t rs = mkrs(P.ws, WS_END), rf = mkrs(P.flags, FLAG_BYTES);
    const int col0 = 32 * q + 8 * g;
    float ga[8];
    if (w == 0) wait_flags(P, rf, FL_MOD + 9 * 4, 5, l, 0x400u);
    __syncthreads();
#pragma unroll
    for (int i = 0; i < 8; ++i) ga[i] = u2f(__builtin_amdgcn_raw_buffer_load_b32(rs, WS_MOD + (unsigned)(2 * DM + col0 + i) * 4u, 0, 16));
    v4u US[2][4], UN[2][4];
#define PU_US(D, S_) do { _Pragma("unroll") for (int i = 0; i < 2; ++i) _Pragma("unroll") for (int k = 0; k < 4; ++k) \
            D[i][k] = ldS(rs, l * 16, WS_US + (unsigned)(2 * q + i) * 32768u + (unsigned)(4 * w + k) * 1024u + (unsigned)(S_) * USEG_STEP); } while (0)
    wait_flags(P, rf, FL_VU + w * 16, 4, l, 0x1000u);
    PU_US(UN, 0);
    const unsigned fo = l < 4 ? FL_UM + (unsigned)(4 * w + l) * 4u : l < 12 ? FL_P + (unsigned)(8 * w + (l - 4)) * 4u : FL_VU + 128u + (unsigned)(4 * w + (l & 3)) * 4u;
    const unsigned fstep = (l >= 4 && l < 12) ? 256u : 128u;
    for (int s = 0; s < NSEG; ++s) {
        v4u xin = {0u, 0u, 0u, 0u};
        if (w < 2) xin = *(const v4u*)(P.x + (size_t)(s * 32 + w * 16 + n) * DM + col0);
        for (int sp = 0;; ++sp) {
            unsigned fl; const unsigned o = fo + fstep * (unsigned)((l < 12 || s + 1 < NSEG) ? s : 0); LD4(fl, rf, o, 0u);
            asm volatile("s_waitcnt vmcnt(0)" : "+v"(fl) :: "memory");
            if (sp == 0 && s > 0 && w < 2 && l == 0) set_flag(rf, FL_X1 + (s - 1) * 256 + (q * 2 + w) * 4);
            if (__all(fl != 0u)) break;
            if (spin_fail(P, sp, l, 0x1000u)) break;
            __builtin_amdgcn_s_sleep(2);
        }
#pragma unroll
        for (int i = 0; i < 2; ++i)
#pragma unroll
            for (int k = 0; k < 4; ++k) US[i][k] = UN[i][k];
        v4u UM[2], PS[2][4], PM[2];
        const unsigned po = WS_PF + (unsigned)s * PF_STEP;
#pragma unroll
        for (int tt = 0; tt < 2; ++tt) {
#pragma unroll
            for (int k = 0; k < 4; ++k) PS[tt][k] = ldS(rs, l * 16, po + (unsigned)(tt * 32 + 4 * w + k) * 1024u);
            PM[tt] = ldS(rs, l * 16, po + 65536u + (unsigned)(tt * 8 + w) * 1024u);
        }
#pragma unroll
        for (int i = 0; i < 2; ++i) UM[i] = ldS(rs, l * 16, WS_UM + (unsigned)(2 * q + i) * 8192u + (unsigned)w * 1024u + (unsigned)s * UMP_STEP);
        __builtin_amdgcn_sched_barrier(0);
        PU_US(UN, s + 1 < NSEG ? s + 1 : s);
        __builtin_amdgcn_sched_barrier(0);
#pragma unroll
        for (int i = 0; i < 2; ++i)
#pragma unroll
            for (int tt = 0; tt < 2; ++tt) {
                f4 c = {0.f, 0.f, 0.f, 0.f};
#pragma unroll
                for (int k = 0; k < 4; ++k) c = mf32(US[i][k], PS[tt][k], c);
                c = mf32(UM[i], PM[tt], c);
                *(LAS f4*)(L + BR_OFF + (s & 1) * 32768 + w * 4096 + (tt * 2 + i) * 1024 + l * 16) = c;
            }
        WGBAR();
        if (w < 2) {
            f4 a0 = {0.f, 0.f, 0.f, 0.f}, a1 = a0;
            const ldsp rp = L + BR_OFF + (s & 1) * 32768 + (w * 2 + (g >> 1)) * 1024 + (n + 32 * (g & 1)) * 16;
#pragma unroll
            for (int k = 0; k < 8; ++k) { a0 += *(LAS f4*)(rp + k * 4096); a1 += *(LAS f4*)(rp + k * 4096 + 256); }
            float y[8];
#pragma unroll
            for (int i = 0; i < 4; ++i) {
                float x0 = (i & 1) ? bhi(xin[i >> 1]) : blo(xin[i >> 1]), x1 = (i & 1) ? bhi(xin[2 + (i >> 1)]) : blo(xin[2 + (i >> 1)]);
                y[i] = rbf(fmaf(ga[i], a0[i], x0)); y[4 + i] = rbf(fmaf(ga[4 + i], a1[i], x1));
            }
            v4u o = {pkbf(y[0], y[1]), pkbf(y[2], y[3]), pkbf(y[4], y[5]), pkbf(y[6], y[7])};
            stS(o, rs, WS_X1 + (unsigned)(w * 18 + q) * 1024u + l * 16, (unsigned)s * TF_STEP);
            float sm = ((y[0] + y[1]) + (y[2] + y[3])) + ((y[4] + y[5]) + (y[6] + y[7]));
            sm = xadd16(sm); sm = xadd32(sm);
            const float mu = sm * (1.f / 32.f);
            float m2 = 0.f;
#pragma unroll
            for (int i = 0; i < 8; ++i) { float d = y[i] - mu; m2 = fmaf(d, d, m2); }
            m2 = xadd16(m2); m2 = xadd32(m2);
            v2u st = {f2u(mu), f2u(m2)};
            if (g == 0) __builtin_amdgcn_raw_buffer_store_b64(st, rs, WS_ST + (unsigned)q * 256u + (unsigned)(w * 16 + n) * 8u, (unsigned)s * ST_STEP, 16);
            if (t == 0 && q == 0) TS(5, s);
        }
    }
    VMWAIT0();
    if (w < 2 && l == 0) set_flag(rf, FL_X1 + (NSEG - 1) * 256 + (q * 2 + w) * 4);
#undef PU_US
}

__device__ __forceinline__ float gelu_tanh(float x) {
    const float u = 0.7978845608028654f * (x + 0.044715f * x * x * x);
    const float e = __builtin_amdgcn_exp2f(-2.885390081777927f * u);
    return x * __builtin_amdgcn_rcpf(1.f + e);
}
constexpr int ST_OFF = 81920;
__device__ __forceinline__ void c1_body(const Params& P, int j, ldsp L) {
    const int t = threadIdx.x, w = __builtin_amdgcn_readfirstlane(t >> 6), l = t & 63, n = l & 15, g = l >> 4;
    const rsrc_t rs = mkrs(P.ws, WS_END), rf = mkrs(P.flags, FLAG_BYTES);
    if (w == 0) wait_flags(P, rf, FL_MOD + 13 * 4, 10, l, 0x400u);
    __syncthreads();
    v4u W[2][18];
#pragma unroll
    for (int m = 0; m < 2; ++m) {
        stage_w128(m == 0 ? P.Wg : P.Wu, FF, j * 128, L, t);
#pragma unroll
        for (int ks = 0; ks < 18; ++ks) W[m][ks] = dfrag(L, 256, ks * 32, (w >> 1) * 64 + (w & 1) * 8, 16, l, 7);
    }
    __syncthreads();
    for (int i = t; i < DM; i += 512) {
        float sb = u2f(__builtin_amdgcn_raw_buffer_load_b32(rs, WS_MOD + (unsigned)(3 * DM + i) * 4u, 0, 16));
        float cb = u2f(__builtin_amdgcn_raw_buffer_load_b32(rs, WS_MOD + (unsigned)(4 * DM + i) * 4u, 0, 16));
        *(LAS u16*)(L + i * 2) = (u16)(f2u(sb) >> 16);
        *(LAS float*)(L + 2048 + i * 4) = rbf(1.f + cb);
    }
    __syncthreads();
    f4 bias[2], cs[2];
#pragma unroll
    for (int m = 0; m < 2; ++m) { bias[m] = (f4){0.f, 0.f, 0.f, 0.f}; cs[m] = bias[m]; }
    const v4u onesb = {0x3F803F80u, 0x3F803F80u, 0x3F803F80u, 0x3F803F80u};
#pragma unroll
    for (int ks = 0; ks < 18; ++ks) {
        const v4u sbf = *(LAS v4u*)(L + (ks * 32 + 8 * g) * 2);
        const f4 g0 = *(LAS f4*)(L + 2048 + (ks * 32 + 8 * g) * 4), g1 = *(LAS f4*)(L + 2048 + (ks * 32 + 8 * g) * 4 + 16);
#pragma unroll
        for (int m = 0; m < 2; ++m) {
            bias[m] = mf32(W[m][ks], sbf, bias[m]);
            v4u x = W[m][ks];
            v4u y = {pkbf(blo(x[0]) * g0[0], bhi(x[0]) * g0[1]), pkbf(blo(x[1]) * g0[2], bhi(x[1]) * g0[3]), pkbf(blo(x[2]) * g1[0], bhi(x[2]) * g1[1]), pkbf(blo(x[3]) * g1[2], bhi(x[3]) * g1[3])};
            asm volatile("" : "+v"(y));
            W[m][ks] = y;
            cs[m] = mf32(y, onesb, cs[m]);
        }
    }
    __syncthreads();
    v4u T[5], TS5;
    bool have = false;
#define C1_LOAD(S_) do { TILE_LOAD(T, WS_X1 + (unsigned)(S_) * TF_STEP); \
        if (w >= 4) T[4] = ldS(rs, WS_ST + (unsigned)(w - 4) * 1024u + l * 16, (unsigned)(S_) * ST_STEP); \
        if (w == 0) TS5 = ldS(rs, WS_ST + 4096u + (l & 31) * 16, (unsigned)(S_) * ST_STEP); } while (0)
    unsigned nfl = 0u;
    if (w < 4) __builtin_amdgcn_s_setprio(1);
    const unsigned afl = FL_ACT + (unsigned)(j * 8 + w) * 4u;
    for (int s = 0; s < NSEG; ++s) {
        const ldsp AT = L + AT_OFF + (s & 1) * 36864, ST = L + ST_OFF + (s & 1) * 5120;
        if (!have) {
            if (s > 0) { VMWAIT0(); if (l == 0) set_flag(rf, afl + (unsigned)(s - 1) * 1280u); }
            if (w == 0) wait_flags(P, rf, FL_X1 + s * 256, 36, l, 0x2000u);
            WGBAR();
            C1_LOAD(s);
            if (w == 0) nfl = s + 1 < NSEG ? __builtin_amdgcn_raw_buffer_load_b32(rf, FL_X1 + (unsigned)(l < 36 ? l : 0) * 4u, (unsigned)(s + 1) * 256u, 16) : 0u;
        }
        TILE_STORE(T);
        if (w >= 4) *(LAS v4u*)(ST + (w - 4) * 1024 + l * 16) = T[4];
        if (w == 0) {
            *(LAS v4u*)(ST + 4096 + (l & 31) * 16) = TS5;
            const bool rdy = __all(nfl != 0u);
            if (l == 0) *(LAS unsigned*)(ST + 4608) = rdy ? 1u : 0u;
        }
        WGBAR();
        if (have) { VMWAIT0(); if (l == 0) set_flag(rf, afl + (unsigned)(s - 1) * 1280u); }
        have = __builtin_amdgcn_readfirstlane(*(LAS unsigned*)(ST + 4608)) != 0u;
        if (have) C1_LOAD(s + 1);
        f4 c[2][2];
#pragma unroll
        for (int m = 0; m < 2; ++m) { c[m][0] = (f4){0.f, 0.f, 0.f, 0.f}; c[m][1] = c[m][0]; }
        KLOOP(c[0][0] = mf32(W[0][ks], b0, c[0][0]); c[1][0] = mf32(W[1][ks], b0, c[1][0]);
              c[0][1] = mf32(W[0][ks], b1, c[0][1]); c[1][1] = mf32(W[1][ks], b1, c[1][1]););
        if (have && w == 0) nfl = s + 2 < NSEG ? __builtin_amdgcn_raw_buffer_load_b32(rf, FL_X1 + (unsigned)(l < 36 ? l : 0) * 4u, (unsigned)(s + 2) * 256u, 16) : 0u;
#pragma unroll
        for (int tt = 0; tt < 2; ++tt) {
            f2 st[5];
#pragma unroll
            for (int i = 0; i < 5; ++i) { const int slot = g + 4 * i; st[i] = *(LAS f2*)(ST + (slot < 18 ? slot : 0) * 256 + (tt * 16 + n) * 8); }
            float sm = 0.f;
#pragma unroll
            for (int i = 0; i < 5; ++i) sm += (g + 4 * i < 18) ? st[i][0] : 0.f;
            sm = xadd16(sm); sm = xadd32(sm);
            const float m = sm * (1.f / 18.f);
            float q = 0.f;
#pragma unroll
            for (int i = 0; i < 5; ++i) { float d = st[i][0] - m; q += (g + 4 * i < 18) ? fmaf(32.f * d, d, st[i][1]) : 0.f; }
            q = xadd16(q); q = xadd32(q);
            const float rstd = __builtin_amdgcn_rsqf(q * (1.f / DM) + 1e-5f);
            float a[4];
#pragma unroll
            for (int r = 0; r < 4; ++r) {
                const float gv = fmaf(rstd, fmaf(-m, cs[0][r], c[0][tt][r]), bias[0][r]);
                const float uv = fmaf(rstd, fmaf(-m, cs[1][r], c[1][tt][r]), bias[1][r]);
                a[r] = gelu_tanh(gv) * uv;
            }
            v2u o = {pkbf(a[0], a[1]), pkbf(a[2], a[3])};
            __builtin_amdgcn_raw_buffer_store_b64(o, rs, l * 8, WS_ACT + (unsigned)(tt * 288 + j * 8 + w) * 512u + (unsigned)s * ACT_STEP, 16);
        }
        if (t == 0 && j == 0) TS(6, s);
    }
    VMWAIT0();
    if (l == 0) set_flag(rf, afl + (unsigned)(NSEG - 1) * 1280u);
#undef C1_LOAD
}

__device__ __forceinline__ void c2_body(const Params& P, int cidx, int tt, ldsp L) {
    const int t = threadIdx.x, w = __builtin_amdgcn_readfirstlane(t >> 6), l = t & 63, n = l & 15, g = l >> 4;
    const rsrc_t rs = mkrs(P.ws, WS_END), rf = mkrs(P.flags, FLAG_BYTES);
    if (w == 0) wait_flags(P, rf, FL_MOD + 22 * 4, 5, l, 0x400u);
    __syncthreads();
    v4u W[2][18];
    {
        const ldsp S = L + w * 15360;
#pragma unroll
        for (int pth = 0; pth < 3; ++pth) {
            v4u tmp[12];
#pragma unroll
            for (int i = 0; i < 12; ++i) { const int ch = l + 64 * i; tmp[i] = *(const v4u*)(P.Wd + (size_t)(w * DM + pth * 192 + (ch >> 2)) * DM + cidx * 32 + (ch & 3) * 8); }
            asm volatile("s_waitcnt lgkmcnt(0)" ::: "memory");
#pragma unroll
            for (int i = 0; i < 12; ++i) { const int ch = l + 64 * i; *(LAS v4u*)(S + (ch >> 2) * 80 + (ch & 3) * 16) = tmp[i]; }
            asm volatile("s_waitcnt lgkmcnt(0)" ::: "memory");
#pragma unroll
            for (int k = 0; k < 6; ++k) { W[0][pth * 6 + k] = dfrag(S, 80, k * 32, 0, 16, l, 0); W[1][pth * 6 + k] = dfrag(S, 80, k * 32, 8, 16, l, 0); }
        }
    }
    float gb[8];
#pragma unroll
    for (int i = 0; i < 8; ++i) gb[i] = u2f(__builtin_amdgcn_raw_buffer_load_b32(rs, WS_MOD + (unsigned)(5 * DM + cidx * 32 + 8 * g + i) * 4u, 0, 16));
    __syncthreads();
    for (int s = 0; s < NSEG; ++s) {
        wait_flags(P, rf, FL_ACT + s * 1280 + (36 * w) * 4, 36, l, 0x4000u);
        const unsigned ao = WS_ACT + (unsigned)(tt * 144 + 18 * w) * 1024u + (unsigned)s * ACT_STEP;
        v4u B[18];
#pragma unroll
        for (int k = 0; k < 18; ++k) {
            v2u x = __builtin_amdgcn_raw_buffer_load_b64(rs, l * 8, ao + (unsigned)k * 1024u, 16), y = __builtin_amdgcn_raw_buffer_load_b64(rs, l * 8 + 512, ao + (unsigned)k * 1024u, 16);
            B[k] = (v4u){x[0], x[1], y[0], y[1]};
        }
        v4u xin = {0u, 0u, 0u, 0u};
        if (w == 0) xin = ldS(rs, WS_X1 + (unsigned)(tt * 18 + cidx) * 1024u + l * 16, (unsigned)s * TF_STEP);
        f4 c0 = {0.f, 0.f, 0.f, 0.f}, c1 = c0;
#pragma unroll
        for (int k = 0; k < 18; ++k) { c0 = mf32(W[0][k], B[k], c0); c1 = mf32(W[1][k], B[k], c1); }
        const ldsp R = L + (s & 1) * 16384;
        *(LAS f4*)(R + w * 2048 + l * 16) = c0; *(LAS f4*)(R + w * 2048 + 1024 + l * 16) = c1;
        __syncthreads();
        if (w == 0) {
            f4 a0 = {0.f, 0.f, 0.f, 0.f}, a1 = a0;
#pragma unroll
            for (int k = 0; k < 8; ++k) { a0 += *(LAS f4*)(R + k * 2048 + l * 16); a1 += *(LAS f4*)(R + k * 2048 + 1024 + l * 16); }
            v4u o;
            o[0] = pkbf(fmaf(gb[0], a0[0], blo(xin[0])), fmaf(gb[1], a0[1], bhi(xin[0])));
            o[1] = pkbf(fmaf(gb[2], a0[2], blo(xin[1])), fmaf(gb[3], a0[3], bhi(xin[1])));
            o[2] = pkbf(fmaf(gb[4], a1[0], blo(xin[2])), fmaf(gb[5], a1[1], bhi(xin[2])));
            o[3] = pkbf(fmaf(gb[6], a1[2], blo(xin[3])), fmaf(gb[7], a1[3], bhi(xin[3])));
            *(v4u*)(P.out + (size_t)(s * 32 + tt * 16 + n) * DM + cidx * 32 + 8 * g) = o;
            if (t == 0 && cidx == 0 && tt == 0) TS(7, s);
        }
    }
}

__global__ void __launch_bounds__(512, 2) fused_kernel(Params P) {
    extern __shared__ __attribute__((aligned(16))) unsigned char lds[];
    const ldsp L = (ldsp)lds;
    if (threadIdx.x == 0) {
        GAS unsigned* f = (GAS unsigned*)P.flags;
        const unsigned xcc = __builtin_amdgcn_s_getreg((31 << 11) | 20) & 0xfu;
        int role = -1;
        const unsigned xt = __hip_atomic_fetch_add(f + FL_TX / 4 + (xcc & 15u) * 32u, 1u, __ATOMIC_RELAXED, __HIP_MEMORY_SCOPE_AGENT);
        if (xcc < 2u) { if (xt < 32u) role = (int)(xcc * 32u + xt); }
        else if (xcc < 4u) { if (xt < 18u) role = (int)(64u + (xcc - 2u) * 18u + xt); }
        if (role < 0) {
            const unsigned tk = __hip_atomic_fetch_add(f + FL_TB / 4, 1u, __ATOMIC_RELAXED, __HIP_MEMORY_SCOPE_AGENT);
            if (tk < (unsigned)(256 - NREC)) role = NREC + (int)tk;
            else {
                int k = (int)tk - (256 - NREC);
                unsigned c[4];
                for (int sp = 0;; ++sp) {
                    unsigned tot = 0u;
                    for (int x = 0; x < 16; ++x) { unsigned v = __hip_atomic_load(f + FL_TX / 4 + x * 32, __ATOMIC_RELAXED, __HIP_MEMORY_SCOPE_AGENT); tot += v; if (x == 0) c[0] = v; if (x == 1) c[1] = v; if (x == 2) c[2] = v; if (x == 3) c[3] = v; }
                    if (tot >= 256u || sp > SPIN_MAX) break;
                }
                const int m0 = c[0] < 32u ? 32 - (int)c[0] : 0, m1 = c[1] < 32u ? 32 - (int)c[1] : 0, m2 = c[2] < 18u ? 18 - (int)c[2] : 0;
                if (k < m0) role = (int)c[0] + k;
                else if (k < m0 + m1) role = 32 + (int)c[1] + (k - m0);
                else if (k < m0 + m1 + m2) role = 64 + (int)c[2] + (k - m0 - m1);
                else role = 82 + (int)c[3] + (k - m0 - m1 - m2);
            }
        }
        *(LAS int*)(L + LDS_BYTES - 32) = role;
    }
    __syncthreads();
    const int role = __builtin_amdgcn_readfirstlane(*(LAS int*)(L + LDS_BYTES - 32));
    if (threadIdx.x == 0 && role < 64) TS(8, role);
    if (role < 32) a_body(P, role, L);
    else if (role < 64) b_body(P, role - 32, L);
    else if (role < NREC) r_body(P, role - 64, L);
    else {
        const int b = role - NREC;
        if (b < 56) {
            if (b < 27) { mod_slice(P, b, L); __syncthreads(); }
            if (b >= 24) { ln1_body(P, b - 24, 32, L); __syncthreads(); }
            if (b < 18) pu_body(P, b, L);
            else if (b < 54) c2_body(P, (b - 18) % 18, (b - 18) / 18, L);
        }
        else if (b < 88) qk_body(P, b - 56, L);
        else if (b < 120) vu_body(P, b - 88, L);
        else {
            c1_body(P, b - 120, L);
        }
    }
}

extern "C" void kernel_launch(void* const* d_in, const int* in_sizes, int n_in, void* d_out, int out_size, void* d_ws, size_t ws_size, hipStream_t stream) {
    Params P;
    P.x = (const u16*)d_in[0]; P.mem0 = (const u16*)d_in[1]; P.Wq = (const u16*)d_in[2]; P.Wk = (const u16*)d_in[3]; P.Wv = (const u16*)d_in[4]; P.Wo = (const u16*)d_in[5];
    P.Wg = (const u16*)d_in[8]; P.Wu = (const u16*)d_in[9]; P.Wd = (const u16*)d_in[10]; P.c = (const u16*)d_in[11]; P.wmod = (const u16*)d_in[12];
    P.out = (u16*)d_out; P.flags = (unsigned*)d_ws;
    P.ws = (unsigned char*)(((size_t)d_ws + FLAG_BYTES + (2u << 20) - 1) & ~(size_t)((2u << 20) - 1));
    (void)hipFuncSetAttribute((const void*)fused_kernel, hipFuncAttributeMaxDynamicSharedMemorySize, LDS_BYTES);
    (void)hipMemsetAsync(d_ws, 0, FLAG_BYTES, stream);
    fused_kernel<<<256, 512, LDS_BYTES, stream>>>(P);
}
```
